```python
import torch

def _softmax(x, axis=-1):
    return torch.softmax(x, dim=axis)
def _gelu(x):
    return torch.nn.functional.gelu(x, approximate="tanh")
def _silu(x):
    return torch.nn.functional.silu(x)
def _sigmoid(x):
    return torch.sigmoid(x)
def _layernorm(x, axis=-1, eps=1e-5):
    m = x.mean(dim=axis, keepdim=True)
    v = x.var(dim=axis, keepdim=True, unbiased=False)
    return (x - m) / torch.sqrt(v + eps)
def _scatter_add(tgt, idx, src, axis=0):
    return tgt.clone().index_add_(axis, idx, src)
def _segment_sum(src, seg, n):
    out = torch.zeros((n,) + src.shape[1:], dtype=src.dtype)
    return out.index_add_(0, seg, src)
def _pad(x, pw):
    flat = tuple(v for pair in reversed(pw) for v in pair)
    return torch.nn.functional.pad(x, flat)

kernel_name = 'gated_mix__mha_block__dense_activation_mish__prompt'

def setup_inputs():

    _w = (lambda a: (lambda t: t.to(torch.bfloat16) if t.is_floating_point() else t)(torch.as_tensor(a)))
    def _A_setup():
        B, S, D, H = 1, 2048, 512, 32
        return {
            "x": _w((lambda *s: torch.randn(*s, dtype=torch.bfloat16) * 0.05)(B, S, D)),
            "wqkv": _w((lambda *s: torch.randn(*s, dtype=torch.bfloat16) * 0.8)(D, 3 * D)),
            "wo": _w((lambda *s: torch.randn(*s, dtype=torch.bfloat16) * 0.05)(D, D)),
        }
    _A_in = _A_setup()

    def _B_setup():
        B, S, D, F = 1, 2048, 512, 1376
        return {
            "x": _w((lambda *s: torch.randn(*s, dtype=torch.bfloat16) * 0.05)(B, S, D)),
            "w1": _w((lambda *s: torch.randn(*s, dtype=torch.bfloat16) * 12.8)(D, F) * (D ** -0.5)),
            "b1": _w((lambda *s: torch.randn(*s, dtype=torch.bfloat16) * 0.2)(F)),
            "w2": _w((lambda *s: torch.randn(*s, dtype=torch.bfloat16) * 0.1)(F, D) * (F ** -0.5)),
        }
    _B_in = _B_setup()

    return {
        "A_x": _A_in["x"],
        "A_wqkv": _A_in["wqkv"],
        "A_wo": _A_in["wo"],
        "B_x": _B_in["x"],
        "B_w1": _B_in["w1"],
        "B_b1": _B_in["b1"],
        "B_w2": _B_in["w2"],
        "gate_w": _w((lambda *s: torch.randn(*s, dtype=torch.bfloat16) * 0.05)(512) * 16.0),
    }

def reference(A_x, A_wqkv, A_wo, B_x, B_w1, B_b1, B_w2, gate_w):
    def _A_ref(x, wqkv, wo):
        B, S, D = x.shape
        H = 32
        Dh = D // H
        qkv = (lambda x, *d: x.permute(*d))((x @ wqkv).reshape(B, S, 3, H, Dh), 2, 0, 3, 1, 4)
        q, k, v = qkv[0], qkv[1], qkv[2]
        scores = torch.einsum("bhqd,bhkd->bhqk", q, k) * (Dh ** -0.5)
        p = _softmax(scores, axis=-1)
        o = torch.einsum("bhqk,bhkd->bhqd", p, v)
        return (lambda x, *d: x.permute(*d))(o, 0, 2, 1, 3).reshape(B, S, D) @ wo

    def _B_ref(x, w1, b1, w2):
        h = torch.einsum("bsd,df->bsf", x, w1) + b1
        sp = torch.log1p(torch.exp(-torch.abs(h))) + torch.where(h > 0, h, torch.zeros_like(h))
        act = h * torch.tanh(sp)
        return torch.einsum("bsf,fd->bsd", act, w2)

    x = A_x
    g = _sigmoid((x * gate_w).sum(-1, keepdims=True))
    a = _A_ref(x, A_wqkv, A_wo)
    b = _B_ref(x, B_w1, B_b1, B_w2)
    return g * a + (1 - g) * b
```

```cpp
#include <hip/hip_runtime.h>
#include <hip/hip_bf16.h>
#include <stdint.h>

typedef __attribute__((ext_vector_type(8))) __bf16 bf16x8;
typedef __attribute__((ext_vector_type(2))) __bf16 bf16x2;
typedef __attribute__((ext_vector_type(2))) float f32x2;
typedef __attribute__((ext_vector_type(4))) float f32x4;
typedef __attribute__((ext_vector_type(16))) float f32x16;
typedef __attribute__((ext_vector_type(4))) unsigned u32x4;
typedef __attribute__((ext_vector_type(2))) unsigned u32x2;
typedef short v4i16_t __attribute__((ext_vector_type(4)));
typedef short v8i16_t __attribute__((ext_vector_type(8)));
typedef unsigned short u16;
#define LAS __attribute__((address_space(3)))
#define GAS __attribute__((address_space(1)))
#define DI __device__ __forceinline__
#define MFMA(a, b, c) __builtin_amdgcn_mfma_f32_32x32x16_bf16((a), (b), (c), 0, 0, 0)

constexpr int S_ = 2048, D_ = 512, H_ = 32, F_ = 1376;
constexpr int NKS_F = F_ / 16;
constexpr int NKS_D = D_ / 16;

constexpr size_t WS_Q = 0;
constexpr size_t WS_K = WS_Q + (size_t)2 * 1024 * 1024;
constexpr size_t WS_V = WS_K + (size_t)2 * 1024 * 1024;
constexpr size_t WS_O = WS_V + (size_t)2 * 1024 * 1024;
constexpr size_t WS_ACT = WS_O + (size_t)2 * 1024 * 1024;
constexpr size_t WS_W2 = WS_ACT + (size_t)64 * NKS_F * 1024;
constexpr size_t WS_WO = WS_W2 + (size_t)16 * NKS_F * 1024;
constexpr size_t WS_G = WS_WO + (size_t)16 * NKS_D * 1024;
constexpr size_t WS_WOX = WS_G + 8192;
constexpr size_t WS_B = WS_WOX + (size_t)8 * 16 * NKS_D * 1024;

DI unsigned pk2(float a, float b) { bf16x2 r = __builtin_convertvector((f32x2){a, b}, bf16x2); return __builtin_bit_cast(unsigned, r); }
DI float bf2f(u16 b) { return __uint_as_float((unsigned)b << 16); }
DI int crow(int r, int h) { return (r & 3) + 8 * (r >> 2) + 4 * h; }
DI u32x4 pack8(const f32x16& x, int s) {
    u32x4 p;
    p[0] = pk2(x[8 * s + 0], x[8 * s + 1]); p[1] = pk2(x[8 * s + 2], x[8 * s + 3]);
    p[2] = pk2(x[8 * s + 4], x[8 * s + 5]); p[3] = pk2(x[8 * s + 6], x[8 * s + 7]);
    return p;
}
DI void glds16(const char* sbase, unsigned voff, unsigned lds_base) {
    unsigned sv;
    asm volatile("s_mov_b32 %0, m0\n\ts_mov_b32 m0, %3\n\ts_nop 0\n\tglobal_load_lds_dwordx4 %1, %2\n\ts_mov_b32 m0, %0"
                 : "=&s"(sv) : "v"(voff), "s"(sbase), "s"(lds_base) : "memory");
}
#define STAMP(ws, kid, idx)
#define WAIT_V(n) asm volatile("s_waitcnt vmcnt(" #n ")" ::: "memory")
#define BAR() __builtin_amdgcn_s_barrier()

DI float mish_f(float h) {
    float u = __builtin_amdgcn_exp2f(-fabsf(h) * 1.4426950408889634f);
    float num, den;
    if (h > 0.f) { num = 1.f + 2.f * u; den = num + 2.f * u * u; }
    else { num = u * (u + 2.f); den = num + 2.f; }
    return h * num * __builtin_amdgcn_rcpf(den);
}

constexpr int K1_SA = 128 * 64 * 2;
constexpr int K1_SB = 3 * 64 * 64 * 2;
constexpr int K1_ST = K1_SA + K1_SB;
constexpr int K1_NBUF = 4;
constexpr int K1_LDS = K1_ST * K1_NBUF;

DI void k1_tile(bool qkv, int grp, int t6, int& type, int& idx) {
    const int j = t6 >> 1, hf = t6 & 1;
    if (qkv) { type = j; idx = grp * 2 + hf; }
    else { idx = (3 * grp + j) * 2 + hf; type = idx < 43 ? 3 : 4; }
}

struct K1Frag { bf16x8 a, b[3]; };

DI void k1_ldfrag(K1Frag& f, const LAS char* base, const int (&aoff)[4], const int (&boff)[3], int ks) {
    f.a = *(const LAS bf16x8*)(base + aoff[ks]);
#pragma unroll
    for (int i = 0; i < 3; ++i) {
        v4i16_t lo = __builtin_amdgcn_ds_read_tr16_b64_v4i16((LAS v4i16_t*)(base + boff[i] + ks * 2048));
        v4i16_t hi = __builtin_amdgcn_ds_read_tr16_b64_v4i16((LAS v4i16_t*)(base + boff[i] + ks * 2048 + 512));
        v8i16_t w = __builtin_shufflevector(lo, hi, 0, 1, 2, 3, 4, 5, 6, 7);
        f.b[i] = __builtin_bit_cast(bf16x8, w);
    }
}
template <int VMASK>
DI void k1_mma(const K1Frag& f, f32x16 (&acc)[3]) {
#pragma unroll
    for (int i = 0; i < 3; ++i) {
        if ((VMASK >> i) & 1) acc[i] = MFMA(f.a, f.b[i], acc[i]); else acc[i] = MFMA(f.b[i], f.a, acc[i]);
    }
}

struct K1Src { const char* abase; unsigned aoff0, aoff1; const char* bb[3]; unsigned bo[3]; int bs; unsigned adst, bdst;
               const char* px; unsigned pxo; const char* pw; int pws; };

DI void k1_prefetch(const K1Src& c, int st, u32x4& d0, u32x4& d1) {
    asm volatile("global_load_dwordx4 %0, %1, %2" : "=v"(d0) : "v"(c.pxo), "s"(c.px + st * 128) : "memory");
    asm volatile("global_load_dwordx4 %0, %1, off" : "=v"(d1) : "v"(c.pw + (size_t)(st * c.pws)) : "memory");
}
#define K1_KEEP(a, b) asm volatile("" :: "v"(a), "v"(b))

DI void k1_issue(const K1Src& c, int st, int buf) {
    glds16(c.abase + st * 128, c.aoff0, c.adst + buf * K1_ST);
    glds16(c.abase + st * 128, c.aoff1, c.adst + buf * K1_ST + 1024);
    glds16(c.bb[0] + (size_t)(st * c.bs), c.bo[0], c.bdst + buf * K1_ST);
    glds16(c.bb[1] + (size_t)(st * c.bs), c.bo[1], c.bdst + buf * K1_ST + 8192);
    glds16(c.bb[2] + (size_t)(st * c.bs), c.bo[2], c.bdst + buf * K1_ST + 16384);
}

#define SB0() __builtin_amdgcn_sched_barrier(0)
template <int VORIENT>
DI void k1_main(LAS char* lds, const K1Src& src, int wid, int lane, f32x16 (&acc)[3], char* ws) {
    const int r = lane & 31, h = lane >> 5;
    const int mtile = wid & 3, nhalf = wid >> 2;
    const int arow = mtile * 32 + r;
    const int asw = (arow >> 1) & 7;
    int aoff[4];
#pragma unroll
    for (int ks = 0; ks < 4; ++ks) aoff[ks] = arow * 128 + (((2 * ks + h) ^ asw) << 4);
    const int g16 = lane >> 4, q = (lane & 15) >> 2, p = lane & 3;
    int boff[3];
#pragma unroll
    for (int i = 0; i < 3; ++i) {
        const int t6 = 3 * nhalf + i;
        boff[i] = K1_SA + (t6 >> 1) * 8192 + (8 * (g16 >> 1) + q) * 128 + (((t6 & 1) ^ (q >> 1)) << 6) + (g16 & 1) * 32 + p * 8;
    }
    K1Frag f0, f1;
    u32x4 pa0, pa1, pb0, pb1;
    WAIT_V(38);
    BAR();
    STAMP(ws, 0, 3);
    k1_ldfrag(f0, lds, aoff, boff, 0);
    k1_ldfrag(f1, lds, aoff, boff, 1);
#pragma unroll
    for (int it = 0; it < 8; ++it) {
        const LAS char* base = lds + (it % K1_NBUF) * K1_ST;
        const LAS char* nbase = lds + ((it + 1) % K1_NBUF) * K1_ST;
        SB0(); k1_mma<VORIENT>(f0, acc); SB0();
        k1_ldfrag(f0, base, aoff, boff, 2);
        SB0(); k1_mma<VORIENT>(f1, acc); SB0();
        k1_ldfrag(f1, base, aoff, boff, 3);
        if (it < 7) {
            if (it < 2) WAIT_V(31); else if (it == 2) WAIT_V(9); else if (it == 3) WAIT_V(7); else if (it < 6) WAIT_V(5); else WAIT_V(0);
            if (it == 3) K1_KEEP(pa0, pa1);
            if (it == 4) K1_KEEP(pb0, pb1);
            BAR();
            if (it + 3 < 8) k1_issue(src, it + 3, (it + 3) % K1_NBUF);
            if (it == 0) k1_prefetch(src, 6, pa0, pa1);
            if (it == 1) k1_prefetch(src, 7, pb0, pb1);
        }
        SB0(); k1_mma<VORIENT>(f0, acc); SB0();
        if (it < 7) k1_ldfrag(f0, nbase, aoff, boff, 0);
        SB0(); k1_mma<VORIENT>(f1, acc); SB0();
        if (it < 7) k1_ldfrag(f1, nbase, aoff, boff, 1);
    }
}

constexpr int K1_NSIDE = 8 + 2 + 12;

__global__ __launch_bounds__(512) void k1_kernel(const u16* __restrict__ x, const u16* __restrict__ wqkv, const u16* __restrict__ w1,
                                                 const u16* __restrict__ b1, const u16* __restrict__ w2, const u16* __restrict__ wo,
                                                 const u16* __restrict__ gw, char* __restrict__ ws) {
    extern __shared__ __attribute__((aligned(16))) char smem[];
    LAS char* lds = (LAS char*)smem;
    const int tid = threadIdx.x, lane = tid & 63;
    const int wid = __builtin_amdgcn_readfirstlane(tid >> 6);
    const int bid = blockIdx.x;
    const int xj = bid & 7, xi = bid >> 3;
    const bool qkv = xi < 16;
    const int mt = qkv ? xi : (xj & 1) * 8 + (xi & 7);
    const int grp = qkv ? xj : (xj >> 1) * 2 + ((xi >> 3) & 1);
    const int r = lane & 31, h = lane >> 5;
    STAMP(ws, 0, 0);

    const int row0 = wid * 16 + (lane >> 3), row1 = row0 + 8;
    K1Src src;
    u32x4 pk3a, pk3b, pk4a, pk4b, pk5a, pk5b;
    src.abase = (const char*)(x + (size_t)(mt * 128) * D_);
    src.aoff0 = (row0 * D_ + (((lane & 7) ^ ((row0 >> 1) & 7)) << 3)) * 2;
    src.aoff1 = (row1 * D_ + (((lane & 7) ^ ((row1 >> 1) & 7)) << 3)) * 2;
    {
        const int kr = wid * 8 + (lane >> 3);
        const int ch = (lane & 7) ^ (((kr >> 1) & 1) << 2);
        const int ld = qkv ? 3 * D_ : F_;
#pragma unroll
        for (int j = 0; j < 3; ++j) {
            const u16* sp; int c = ch;
            if (qkv) sp = wqkv + j * D_ + 64 * grp;
            else { int P = 3 * grp + j; if (P > 21) P = 20; if (P == 21) c &= 3; sp = w1 + 64 * P; }
            src.bb[j] = (const char*)sp;
            src.bo[j] = (kr * ld + c * 8) * 2;
        }
        src.bs = 128 * ld;
        const int gw8 = xi * 8 + wid;
        src.px = (const char*)(x + (size_t)(gw8 * 8) * D_);
        src.pxo = ((lane >> 3) * D_ + (lane & 7) * 8) * 2;
        int sg = gw8 * 3 + ((lane >> 3) < 3 ? (lane >> 3) : 0); if (sg >= 576) sg -= 576;
        const int pc = sg >> 6, pk = sg & 63;
        int P = 3 * ((xj >> 1) * 2) + (pc - 3); if (P > 21) P = 20;
        const int pcl = (P == 21) ? (lane & 3) : (lane & 7);
        const u16* pb = pc < 3 ? wqkv + pc * D_ + 64 * xj + (size_t)pk * (3 * D_) + (lane & 7) * 8
                               : w1 + 64 * P + (size_t)pk * F_ + pcl * 8;
        src.pw = (const char*)pb;
        src.pws = pc < 3 ? 128 * 3 * D_ : 128 * F_;
    }
    const unsigned lds0 = (unsigned)(uintptr_t)lds;
    src.adst = lds0 + wid * 2048;
    src.bdst = lds0 + K1_SA + wid * 1024;
    {
        u32x4 p0a, p0b, p1a, p1b, p2a, p2b, p3a, p3b, p4a, p4b, p5a, p5b;
        k1_prefetch(src, 0, p0a, p0b); k1_prefetch(src, 1, p1a, p1b); k1_prefetch(src, 2, p2a, p2b);
        WAIT_V(4); K1_KEEP(p0a, p0b);
        k1_issue(src, 0, 0); k1_prefetch(src, 3, p3a, p3b);
        WAIT_V(9); K1_KEEP(p1a, p1b);
        k1_issue(src, 1, 1); k1_prefetch(src, 4, p4a, p4b);
        WAIT_V(14); K1_KEEP(p2a, p2b);
        k1_issue(src, 2, 2); k1_prefetch(src, 5, p5a, p5b);
        pk3a = p3a; pk3b = p3b; pk4a = p4a; pk4b = p4b; pk5a = p5a; pk5b = p5b;
    }

    const int gwv = bid * 8 + wid;
    u16 rp[8];
    {
        int f = gwv < 16 * NKS_F + 16 * NKS_D ? gwv : 0;
        const u16* wsrc; int nt, ks;
        if (f < 16 * NKS_F) { wsrc = w2; nt = f / NKS_F; ks = f % NKS_F; }
        else { f -= 16 * NKS_F; wsrc = wo; nt = f / NKS_D; ks = f % NKS_D; }
#pragma unroll
        for (int j = 0; j < 8; ++j) rp[j] = wsrc[(size_t)(16 * ks + 8 * (j >> 2) + 4 * h + (j & 3)) * D_ + nt * 32 + r];
    }
    const u32x4 gx = *(const u32x4*)(x + (size_t)gwv * D_ + lane * 8);
    const u32x4 gg = *(const u32x4*)(gw + lane * 8);
    int tty[3], tix[3];
    u32x2 bias[3][4];
#pragma unroll
    for (int i = 0; i < 3; ++i) {
        k1_tile(qkv, grp, 3 * (wid >> 2) + i, tty[i], tix[i]);
        const int bi = tty[i] == 3 ? tix[i] : 0;
#pragma unroll
        for (int qd = 0; qd < 4; ++qd) bias[i][qd] = *(const u32x2*)(b1 + 32 * bi + 8 * qd + 4 * h);
    }
    asm volatile("" ::: "memory");

    f32x16 acc[3];
#pragma unroll
    for (int i = 0; i < 3; ++i) acc[i] = (f32x16){};
    if (qkv && wid >= 4) k1_main<6>(lds, src, wid, lane, acc, ws);
    else k1_main<0>(lds, src, wid, lane, acc, ws);
    static_assert(K1_NSIDE + 16 == 38, "vmcnt of the first stage");
    K1_KEEP(pk3a, pk3b); K1_KEEP(pk4a, pk4b); K1_KEEP(pk5a, pk5b);

    STAMP(ws, 0, 1);
    const int tt = mt * 4 + (wid & 3);
#pragma unroll
    for (int i = 0; i < 3; ++i) {
        const int ty = tty[i], ix = tix[i];
        if (ty == 0 || ty == 1) {
            f32x16 a = acc[i];
            if (ty == 0) a = a * (0.25f * 1.4426950408889634f);
            char* dst = ws + (ty == 0 ? WS_Q : WS_K);
#pragma unroll
            for (int s = 0; s < 2; ++s)
                *(u32x4*)(dst + ((size_t)(2 * ix + s) * 64 + tt) * 1024 + lane * 16) = pack8(a, s);
        } else if (ty == 2) {
            const int head = 2 * ix + (r >> 4), d = r & 15;
#pragma unroll
            for (int s = 0; s < 2; ++s)
                *(u32x4*)(ws + WS_V + ((size_t)head * 128 + 2 * tt + s) * 512 + h * 256 + d * 16) = pack8(acc[i], s);
        } else if (ty == 3) {
            f32x16 a = acc[i];
#pragma unroll
            for (int e = 0; e < 16; ++e) {
                const unsigned bw = bias[i][e >> 2][(e >> 1) & 1];
                a[e] = mish_f(a[e] + __uint_as_float((e & 1) ? (bw & 0xffff0000u) : (bw << 16)));
            }
#pragma unroll
            for (int s = 0; s < 2; ++s)
                *(u32x4*)(ws + WS_ACT + ((size_t)tt * NKS_F + 2 * ix + s) * 1024 + lane * 16) = pack8(a, s);
        }
    }
    if (gwv < 16 * NKS_F + 16 * NKS_D) {
        u32x4 o;
#pragma unroll
        for (int j = 0; j < 4; ++j) o[j] = (unsigned)rp[2 * j] | ((unsigned)rp[2 * j + 1] << 16);
        *(u32x4*)(ws + WS_W2 + (size_t)gwv * 1024 + lane * 16) = o;
    }
    {
        float sacc = 0.f;
#pragma unroll
        for (int j = 0; j < 4; ++j) {
            sacc += __uint_as_float(gx[j] << 16) * __uint_as_float(gg[j] << 16);
            sacc += __uint_as_float(gx[j] & 0xffff0000u) * __uint_as_float(gg[j] & 0xffff0000u);
        }
#pragma unroll
        for (int o = 32; o > 0; o >>= 1) sacc += __shfl_xor(sacc, o);
        if (lane == 0) ((float*)(ws + WS_G))[gwv] = 1.f / (1.f + __expf(-sacc));
    }
    STAMP(ws, 0, 2);
}

#define X_NOLDS 0
#define X_NOMLP 0
constexpr int K2_CONST = 128 * 1024;
constexpr int K2_RED = K2_CONST + 20 * 512;
constexpr int K2_FLAG = K2_RED + 16384;
constexpr int K2_LDS = K2_FLAG + 16;
constexpr float K2_LIM = 65536.f;
constexpr int K2_PF = 6;

#define A_MFMA_S(D, K) asm volatile("v_mfma_f32_32x32x16_bf16 %0, %1, %2, %3" : "=&v"(D) : "v"(K), "v"(qf), "v"(negm) : "memory")
#define A_MFMA_PV(V, P) asm volatile("v_mfma_f32_32x32x16_bf16 %0, %1, %2, %0" : "+v"(og) : "v"(V), "v"(P))
#define A_MFMA_PV0(V, P) asm volatile("v_mfma_f32_32x32x16_bf16 %0, %1, %2, 0" : "=&v"(og) : "v"(V), "v"(P))
#define A_EXP4(E, S, i) asm volatile("v_exp_f32 %0, %4\n\tv_exp_f32 %1, %5\n\tv_exp_f32 %2, %6\n\tv_exp_f32 %3, %7" \
        : "=&v"(E[i]), "=&v"(E[i + 1]), "=&v"(E[i + 2]), "=&v"(E[i + 3]) : "v"(S[i]), "v"(S[i + 1]), "v"(S[i + 2]), "v"(S[i + 3]))
#define A_CVT4(P, E, i) asm volatile("v_cvt_pk_bf16_f32 %0, %4, %5\n\tv_cvt_pk_bf16_f32 %1, %6, %7\n\tv_cvt_pk_bf16_f32 %2, %8, %9\n\tv_cvt_pk_bf16_f32 %3, %10, %11" \
        : "=&v"(P[0]), "=&v"(P[1]), "=&v"(P[2]), "=&v"(P[3]) \
        : "v"(E[i]), "v"(E[i + 1]), "v"(E[i + 2]), "v"(E[i + 3]), "v"(E[i + 4]), "v"(E[i + 5]), "v"(E[i + 6]), "v"(E[i + 7]))

__global__ __launch_bounds__(768) void k2_kernel(char* __restrict__ ws) {
    extern __shared__ __attribute__((aligned(16))) char smem[];
    LAS char* lds = (LAS char*)smem;
    const int tid = threadIdx.x, lane = tid & 63;
    const int wid = __builtin_amdgcn_readfirstlane(tid >> 6);
    const int bid = blockIdx.x;
    const int r = lane & 31, h = lane >> 5;
    STAMP(ws, 1, 0);

    if (wid >= 8) {
        const int tb = (bid & 7) * 4 + ((bid >> 3) & 3), nb = bid >> 5;
        const int ni = wid & 1, kh = (wid >> 1) & 1;
        const char* wf = ws + WS_W2 + ((size_t)(nb * 2 + ni) * NKS_F + kh * 43) * 1024 + lane * 16;
        const char* af = ws + WS_ACT + ((size_t)(tb * 2) * NKS_F + kh * 43) * 1024 + lane * 16;
        if (tid == 512) { *(LAS u32x4*)(lds + K2_FLAG) = (u32x4){0u, 0u, 0u, 0u}; }
        asm volatile("s_waitcnt lgkmcnt(0)" ::: "memory");
        BAR();
        {
            const size_t o = (size_t)(((bid >> 3) * 4 + (wid & 3)) * 4) * 1024 + lane * 16;
            u32x4 c[4];
#pragma unroll
            for (int i = 0; i < 4; ++i) c[i] = *(const u32x4*)(ws + WS_WO + o + i * 1024);
#pragma unroll
            for (int i = 0; i < 4; ++i) *(u32x4*)(ws + WS_WOX + (size_t)(bid & 7) * (16 * NKS_D * 1024) + o + i * 1024) = c[i];
        }
        int kk = (nb * 5 + (tb & 3) * 11) % 43;
        bf16x8 fw[K2_PF], fa0[K2_PF], fa1[K2_PF];
#pragma unroll
        for (int i = 0; i < K2_PF; ++i) {
            fw[i] = *(const bf16x8*)(wf + kk * 1024);
            fa0[i] = *(const bf16x8*)(af + kk * 1024);
            fa1[i] = *(const bf16x8*)(af + (NKS_F + kk) * 1024);
            kk = kk == 42 ? 0 : kk + 1;
        }
        f32x16 c0 = (f32x16){}, c1 = (f32x16){};
        for (int rd = 0; rd < 7; ++rd) {
            if (rd == 1) BAR();
#pragma unroll
            for (int i = 0; i < K2_PF; ++i) {
                c0 = MFMA(fw[i], fa0[i], c0);
                c1 = MFMA(fw[i], fa1[i], c1);
                fw[i] = *(const bf16x8*)(wf + kk * 1024);
                fa0[i] = *(const bf16x8*)(af + kk * 1024);
                fa1[i] = *(const bf16x8*)(af + (NKS_F + kk) * 1024);
                kk = kk == 42 ? 0 : kk + 1;
            }
        }
        c0 = MFMA(fw[0], fa0[0], c0);
        c1 = MFMA(fw[0], fa1[0], c1);
        LAS f32x4* red = (LAS f32x4*)(lds + K2_RED + ni * 8192);
        volatile LAS unsigned* flag = (volatile LAS unsigned*)(lds + K2_FLAG) + ni;
        if (kh == 1) {
#pragma unroll
            for (int qd = 0; qd < 4; ++qd) {
                red[qd * 64 + lane] = (f32x4){c0[4 * qd], c0[4 * qd + 1], c0[4 * qd + 2], c0[4 * qd + 3]};
                red[(4 + qd) * 64 + lane] = (f32x4){c1[4 * qd], c1[4 * qd + 1], c1[4 * qd + 2], c1[4 * qd + 3]};
            }
            asm volatile("s_waitcnt lgkmcnt(0)" ::: "memory");
            if (lane == 0) *flag = 1u;
        } else {
            const float* gp = (const float*)(ws + WS_G) + tb * 64;
            const float og0 = 1.f - gp[r], og1 = 1.f - gp[32 + r];
            while (*flag == 0u) __builtin_amdgcn_s_sleep(2);
            asm volatile("" ::: "memory");
            f32x4* dst = (f32x4*)(ws + WS_B) + ((size_t)(tb * 8 + nb) * 16 + ni * 8) * 64 + lane;
#pragma unroll
            for (int qd = 0; qd < 4; ++qd) {
                f32x4 u0 = red[qd * 64 + lane], u1 = red[(4 + qd) * 64 + lane];
                u0 += (f32x4){c0[4 * qd], c0[4 * qd + 1], c0[4 * qd + 2], c0[4 * qd + 3]};
                u1 += (f32x4){c1[4 * qd], c1[4 * qd + 1], c1[4 * qd + 2], c1[4 * qd + 3]};
                dst[qd * 64] = u0 * og0;
                dst[(4 + qd) * 64] = u1 * og1;
            }
        }
        STAMP(ws, 1, 3);
        return;
    }

    const int head = (bid & 7) * 4 + ((bid >> 3) & 3), qb = bid >> 5;
    const int qt = qb * 8 + wid;
    bf16x8 qf;
    asm volatile("global_load_dwordx4 %0, %1, %2" : "=v"(qf) : "v"(lane * 16), "s"(ws + WS_Q + ((size_t)head * 64 + qt) * 1024) : "memory");
    {
        const char* kg = ws + WS_K + (size_t)head * 65536 + wid * 1024;
        const char* vg = ws + WS_V + (size_t)head * 65536 + wid * 1024;
        const unsigned lds0 = (unsigned)(uintptr_t)lds + wid * 1024;
#pragma unroll
        for (int i = 0; i < 8; ++i) {
            glds16(kg + i * 8192, lane * 16, lds0 + i * 8192);
            glds16(vg + i * 8192, lane * 16, lds0 + 65536 + i * 8192);
        }
    }
    if (tid < 40) {
        const unsigned c = (tid & 1) ? 0u : 0x3f803f80u;
        *(LAS u32x4*)(lds + K2_CONST + (tid >> 1) * 512 + (tid & 1) * 16) = (u32x4){c, c, c, c};
    }
    const bool vact = r < 16;
    const LAS char* kp = lds + lane * 16;
    const LAS char* vp = vact ? lds + 65536 + h * 256 + r * 16 : lds + K2_CONST + (r == 16 ? 0 : 16);
    const int vinc = vact ? 8192 : 0;

    f32x16 ot = (f32x16){};
    f32x16 negm, sa, sb;
    bf16x8 ka, kb, va0, va1, vb0, vb1;
    if (X_NOLDS) { vb0 = *(const LAS bf16x8*)(vp); vb1 = *(const LAS bf16x8*)(vp + 512); }
    float m;
    asm volatile("s_waitcnt vmcnt(12) lgkmcnt(0)" : "+v"(qf) :: "memory");
    BAR();
    STAMP(ws, 1, 1);
    {
        ka = *(const LAS bf16x8*)kp;
        kb = *(const LAS bf16x8*)(kp + 1024);
        va0 = *(const LAS bf16x8*)(vp); va1 = *(const LAS bf16x8*)(vp + 512);
        f32x16 s = MFMA(ka, qf, (f32x16){});
        float mx = fmaxf(s[0], s[1]);
#pragma unroll
        for (int e = 2; e < 16; ++e) mx = fmaxf(mx, s[e]);
        mx = fmaxf(mx, __shfl_xor(mx, 32));
        m = mx;
#pragma unroll
        for (int e = 0; e < 16; ++e) negm[e] = -m;
        asm volatile("s_nop 3" : "+v"(negm));
        A_MFMA_S(sa, ka);
        asm volatile("s_nop 7\n\ts_nop 7" : "+v"(sa));
    }
    const LAS char* kq = kp;
    const LAS char* vq = vp;
    int g = 0;
    for (; g < 8; ++g) {
        if (g == 1) { WAIT_V(0); BAR(); }
        if (g == 0) __builtin_amdgcn_s_setprio(3);
        else if (g == 4) __builtin_amdgcn_s_setprio(2);
        else if (g == 6) __builtin_amdgcn_s_setprio(1);
        else if (g == 7) __builtin_amdgcn_s_setprio(0);
        const LAS char* kq2 = (g == 7) ? kp - 8192 : kq;
        const LAS char* vq2 = (g == 7) ? vp - vinc : vq;
        asm volatile("" : "+v"(kq), "+v"(vq), "+v"(kq2), "+v"(vq2));
        f32x16 og;
        float e[16];
        u32x4 p0, p1;
#define K2_TILE(SC, SN, KC, KN, VC0, VP1, VN0, VN1, t) do { \
            if ((t) > 0) A_MFMA_PV(VP1, p1); \
            if (!X_NOLDS) { \
            KN = ((t) + 2 < 8) ? *(const LAS bf16x8*)(kq + ((t) + 2) * 1024) : *(const LAS bf16x8*)(kq2 + ((t) + 2) * 1024); \
            if ((t) + 1 < 8) { VN0 = *(const LAS bf16x8*)(vq + ((t) + 1) * 1024); VN1 = *(const LAS bf16x8*)(vq + ((t) + 1) * 1024 + 512); } \
            else { VN0 = *(const LAS bf16x8*)(vq2 + ((t) + 1) * 1024); VN1 = *(const LAS bf16x8*)(vq2 + ((t) + 1) * 1024 + 512); } } \
            A_EXP4(e, SC, 0); A_EXP4(e, SC, 4); \
            A_MFMA_S(SN, KC); \
            A_CVT4(p0, e, 0); \
            A_EXP4(e, SC, 8); \
            if ((t) == 0) A_MFMA_PV0(VC0, p0); else A_MFMA_PV(VC0, p0); \
            A_EXP4(e, SC, 12); \
            A_CVT4(p1, e, 8); \
            asm volatile("s_nop 1"); \
        } while (0)
#pragma unroll
        for (int t = 0; t < 8; t += 2) {
            K2_TILE(sa, sb, kb, ka, va0, vb1, vb0, vb1, t);
            K2_TILE(sb, sa, ka, kb, vb0, va1, va0, va1, t + 1);
        }
        A_MFMA_PV(vb1, p1);
        asm volatile("s_nop 7\n\ts_nop 7\n\ts_nop 3" : "+v"(og));
#undef K2_TILE
        if (__builtin_expect(__any(!(og[8] <= K2_LIM)), 0)) break;
#pragma unroll
        for (int i = 0; i < 9; ++i) ot[i] += og[i];
        kq += 8192; vq += vinc;
    }
    if (__builtin_expect(g < 8, 0)) {
        if (g == 0) { WAIT_V(0); BAR(); }
        for (int t = g * 8; t < 64; ++t) {
            bf16x8 kf = *(const LAS bf16x8*)(kp + t * 1024);
            const LAS char* vt = vp + (vact ? t * 1024 : 0);
            vb0 = *(const LAS bf16x8*)(vt); vb1 = *(const LAS bf16x8*)(vt + 512);
            f32x16 s = MFMA(kf, qf, (f32x16){});
            float mx = fmaxf(s[0], s[1]);
#pragma unroll
            for (int i = 2; i < 16; ++i) mx = fmaxf(mx, s[i]);
            mx = fmaxf(mx, __shfl_xor(mx, 32));
            float mn = fmaxf(m, mx);
            float al = __builtin_amdgcn_exp2f(m - mn);
            m = mn;
            ot = ot * al;
#pragma unroll
            for (int i = 0; i < 16; ++i) s[i] = __builtin_amdgcn_exp2f(s[i] - m);
            ot = MFMA(vb0, __builtin_bit_cast(bf16x8, pack8(s, 0)), ot);
            ot = MFMA(vb1, __builtin_bit_cast(bf16x8, pack8(s, 1)), ot);
        }
    }
    STAMP(ws, 1, 2);
    float l = __shfl(ot[8], r);
    float rl = 1.f / l;
    f32x16 on = ot * rl;
    *(u32x4*)(ws + WS_O + ((size_t)qt * NKS_D + head) * 1024 + lane * 16) = pack8(on, 0);
    STAMP(ws, 1, 3);
}

constexpr int K3_LDS = 64 * 1024;

#define A_GLOAD(D, voff, sbase, imm) asm volatile("global_load_dwordx4 %0, %1, %2 offset:" #imm : "=v"(D) : "v"(voff), "s"(sbase) : "memory")

__global__ __launch_bounds__(256) void k3_kernel(const char* __restrict__ ws, u16* __restrict__ out) {
    extern __shared__ __attribute__((aligned(16))) char smem[];
    const int tid = threadIdx.x, lane = tid & 63;
    const int wid = __builtin_amdgcn_readfirstlane(tid >> 6);
    const int bid = blockIdx.x;
    const int tt = (bid & 7) * 8 + ((bid >> 3) & 7), nq = bid >> 6;
    const int r = lane & 31, h = lane >> 5;
    STAMP(ws, 2, 0);

    const char* af = ws + WS_O + ((size_t)tt * NKS_D + wid * 8) * 1024;
    const char* wf = ws + WS_WOX + (size_t)(bid & 7) * (16 * NKS_D * 1024) + ((size_t)(nq * 4) * NKS_D + wid * 8) * 1024;
    const unsigned vo = lane * 16, vo4 = lane * 16 + 4096;
    bf16x8 a[8], w[4][8];
    A_GLOAD(a[0], vo, af, 0); A_GLOAD(a[1], vo, af, 1024); A_GLOAD(a[2], vo, af, 2048); A_GLOAD(a[3], vo, af, 3072);
    A_GLOAD(a[4], vo4, af, 0); A_GLOAD(a[5], vo4, af, 1024); A_GLOAD(a[6], vo4, af, 2048); A_GLOAD(a[7], vo4, af, 3072);
#pragma unroll
    for (int t = 0; t < 4; ++t) {
        const char* wt = wf + (size_t)t * NKS_D * 1024;
        A_GLOAD(w[t][0], vo, wt, 0); A_GLOAD(w[t][1], vo, wt, 1024); A_GLOAD(w[t][2], vo, wt, 2048); A_GLOAD(w[t][3], vo, wt, 3072);
        A_GLOAD(w[t][4], vo4, wt, 0); A_GLOAD(w[t][5], vo4, wt, 1024); A_GLOAD(w[t][6], vo4, wt, 2048); A_GLOAD(w[t][7], vo4, wt, 3072);
    }
    const int nt = nq * 4 + wid;
    const f32x4* bp = (const f32x4*)(ws + WS_B) + ((size_t)((tt >> 1) * 8 + (nt >> 1)) * 16 + ((nt & 1) * 2 + (tt & 1)) * 4) * 64 + lane;
    f32x4 bq[4];
#pragma unroll
    for (int qd = 0; qd < 4; ++qd) bq[qd] = bp[qd * 64];
    const float g = ((const float*)(ws + WS_G))[tt * 32 + r];

    f32x16 acc[4];
#pragma unroll
    for (int t = 0; t < 4; ++t) acc[t] = (f32x16){};
#define K3_W(t, n) asm volatile("s_waitcnt vmcnt(" #n ")" : "+v"(w[t][0]), "+v"(w[t][1]), "+v"(w[t][2]), "+v"(w[t][3]), "+v"(w[t][4]), "+v"(w[t][5]), "+v"(w[t][6]), "+v"(w[t][7]) :: "memory")
    asm volatile("s_waitcnt vmcnt(37)" : "+v"(a[0]), "+v"(a[1]), "+v"(a[2]), "+v"(a[3]), "+v"(a[4]), "+v"(a[5]), "+v"(a[6]), "+v"(a[7]) :: "memory");
    K3_W(0, 29);
#pragma unroll
    for (int i = 0; i < 8; ++i) acc[0] = MFMA(w[0][i], a[i], acc[0]);
    K3_W(1, 21);
#pragma unroll
    for (int i = 0; i < 8; ++i) acc[1] = MFMA(w[1][i], a[i], acc[1]);
    K3_W(2, 13);
#pragma unroll
    for (int i = 0; i < 8; ++i) acc[2] = MFMA(w[2][i], a[i], acc[2]);
    K3_W(3, 5);
#pragma unroll
    for (int i = 0; i < 8; ++i) acc[3] = MFMA(w[3][i], a[i], acc[3]);
#undef K3_W
    STAMP(ws, 2, 1);
    f32x4* red = (f32x4*)smem;
#pragma unroll
    for (int t = 0; t < 4; ++t)
#pragma unroll
        for (int qd = 0; qd < 4; ++qd)
            red[((wid * 4 + t) * 4 + qd) * 64 + lane] = (f32x4){acc[t][4 * qd], acc[t][4 * qd + 1], acc[t][4 * qd + 2], acc[t][4 * qd + 3]};
    __syncthreads();
    const int tok = tt * 32 + r;
#pragma unroll
    for (int qd = 0; qd < 4; ++qd) {
        f32x4 sacc = red[(wid * 4 + qd) * 64 + lane];
#pragma unroll
        for (int k = 1; k < 4; ++k) sacc += red[((k * 4 + wid) * 4 + qd) * 64 + lane];
        sacc = sacc * g + bq[qd];
        const int n = nt * 32 + 8 * qd + 4 * h;
        u32x2 o = {pk2(sacc[0], sacc[1]), pk2(sacc[2], sacc[3])};
        *(u32x2*)(out + (size_t)tok * D_ + n) = o;
    }
    STAMP(ws, 2, 2);
}

extern "C" void kernel_launch(void* const* d_in, const int* in_sizes, int n_in,
                              void* d_out, int out_size, void* d_ws, size_t ws_size,
                              hipStream_t stream) {
    const u16* x = (const u16*)d_in[0];
    const u16* wqkv = (const u16*)d_in[1];
    const u16* wo = (const u16*)d_in[2];
    const u16* w1 = (const u16*)d_in[4];
    const u16* b1 = (const u16*)d_in[5];
    const u16* w2 = (const u16*)d_in[6];
    const u16* gw = (const u16*)d_in[7];
    char* ws = (char*)d_ws;
    (void)hipFuncSetAttribute((const void*)k1_kernel, hipFuncAttributeMaxDynamicSharedMemorySize, K1_LDS);
    (void)hipFuncSetAttribute((const void*)k2_kernel, hipFuncAttributeMaxDynamicSharedMemorySize, K2_LDS);
    (void)hipFuncSetAttribute((const void*)k3_kernel, hipFuncAttributeMaxDynamicSharedMemorySize, K3_LDS);
    k1_kernel<<<256, 512, K1_LDS, stream>>>(x, wqkv, w1, b1, w2, wo, gw, ws);
    k2_kernel<<<256, X_NOMLP ? 512 : 768, K2_LDS, stream>>>(ws);
    k3_kernel<<<256, 256, K3_LDS, stream>>>(ws, (u16*)d_out);
}
```

```cpp
#include <hip/hip_runtime.h>
#include <hip/hip_bf16.h>
#include <stdint.h>

typedef __attribute__((ext_vector_type(8))) __bf16 bf16x8;
typedef __attribute__((ext_vector_type(2))) __bf16 bf16x2;
typedef __attribute__((ext_vector_type(2))) float f32x2;
typedef __attribute__((ext_vector_type(4))) float f32x4;
typedef __attribute__((ext_vector_type(16))) float f32x16;
typedef __attribute__((ext_vector_type(4))) unsigned u32x4;
typedef __attribute__((ext_vector_type(2))) unsigned u32x2;
typedef short v4i16_t __attribute__((ext_vector_type(4)));
typedef short v8i16_t __attribute__((ext_vector_type(8)));
typedef unsigned short u16;
#define LAS __attribute__((address_space(3)))
#define GAS __attribute__((address_space(1)))
#define DI __device__ __forceinline__
#define MFMA(a, b, c) __builtin_amdgcn_mfma_f32_32x32x16_bf16((a), (b), (c), 0, 0, 0)

constexpr int S_ = 2048, D_ = 512, H_ = 32, F_ = 1376;
constexpr int NKS_F = F_ / 16;
constexpr int NKS_D = D_ / 16;

constexpr size_t WS_Q = 0;
constexpr size_t WS_K = WS_Q + (size_t)2 * 1024 * 1024;
constexpr size_t WS_V = WS_K + (size_t)2 * 1024 * 1024;
constexpr size_t WS_O = WS_V + (size_t)2 * 1024 * 1024;
constexpr size_t WS_ACT = WS_O + (size_t)2 * 1024 * 1024;
constexpr size_t WS_W2 = WS_ACT + (size_t)64 * NKS_F * 1024;
constexpr size_t WS_WO = WS_W2 + (size_t)16 * NKS_F * 1024;
constexpr size_t WS_W1 = WS_WO + (size_t)16 * NKS_D * 1024;
constexpr size_t WS_XF = WS_W1 + (size_t)43 * NKS_D * 1024;
constexpr size_t WS_CNT = WS_XF + (size_t)64 * NKS_D * 1024;
constexpr size_t WS_G = WS_CNT + 32 * 256;
constexpr size_t WS_WOX = WS_G + 8192;
constexpr size_t WS_B = WS_WOX + (size_t)8 * 16 * NKS_D * 1024;

DI unsigned pk2(float a, float b) { bf16x2 r = __builtin_convertvector((f32x2){a, b}, bf16x2); return __builtin_bit_cast(unsigned, r); }
DI float bf2f(u16 b) { return __uint_as_float((unsigned)b << 16); }
DI int crow(int r, int h) { return (r & 3) + 8 * (r >> 2) + 4 * h; }
DI u32x4 pack8(const f32x16& x, int s) {
    u32x4 p;
    p[0] = pk2(x[8 * s + 0], x[8 * s + 1]); p[1] = pk2(x[8 * s + 2], x[8 * s + 3]);
    p[2] = pk2(x[8 * s + 4], x[8 * s + 5]); p[3] = pk2(x[8 * s + 6], x[8 * s + 7]);
    return p;
}
DI void glds16(const char* sbase, unsigned voff, unsigned lds_base) {
    unsigned sv;
    asm volatile("s_mov_b32 %0, m0\n\ts_mov_b32 m0, %3\n\ts_nop 0\n\tglobal_load_lds_dwordx4 %1, %2\n\ts_mov_b32 m0, %0"
                 : "=&s"(sv) : "v"(voff), "s"(sbase), "s"(lds_base) : "memory");
}
#define STAMP(ws, kid, idx)
#define STAMPL(i)
#define STAMPL_FLUSH(ws, kid)
#define WAIT_V(n) asm volatile("s_waitcnt vmcnt(" #n ")" ::: "memory")
#define BAR() __builtin_amdgcn_s_barrier()

DI float mish_f(float h) {
    const float n = __builtin_amdgcn_exp2f(fminf(h, 20.f) * 1.4426950408889634f);
    const float w = n * (n + 2.f);
    return h * w * __builtin_amdgcn_rcpf(w + 2.f);
}

constexpr int K1_SA = 64 * 64 * 2;
constexpr int K1_SB = 3 * 64 * 64 * 2;
constexpr int K1_ST = K1_SA + K1_SB;
constexpr int K1_NBUF = 4;
constexpr int K1_SCR = K1_ST * K1_NBUF;
constexpr int K1_LDS = K1_SCR + 16384;
constexpr int NFRAG = 16 * NKS_F + 16 * NKS_D + 43 * NKS_D;

#define A_MFMA_ACC(C, A, B) asm volatile("v_mfma_f32_32x32x16_bf16 %0, %1, %2, %0" : "+v"(C) : "v"(A), "v"(B))

struct K1Frag { bf16x8 a[4], b[4]; };
DI void k1_ldfrag(K1Frag& f, const LAS char* base, const int (&aoff)[4], int boff) {
#pragma unroll
    for (int ks = 0; ks < 4; ++ks) {
        f.a[ks] = *(const LAS bf16x8*)(base + aoff[ks]);
        v4i16_t lo = __builtin_amdgcn_ds_read_tr16_b64_v4i16((LAS v4i16_t*)(base + boff + ks * 2048));
        v4i16_t hi = __builtin_amdgcn_ds_read_tr16_b64_v4i16((LAS v4i16_t*)(base + boff + ks * 2048 + 512));
        v8i16_t w = __builtin_shufflevector(lo, hi, 0, 1, 2, 3, 4, 5, 6, 7);
        f.b[ks] = __builtin_bit_cast(bf16x8, w);
    }
}

__global__ __launch_bounds__(768) void k1_kernel(const u16* __restrict__ x, const u16* __restrict__ wqkv, const u16* __restrict__ w1,
                                                 const u16* __restrict__ w2, const u16* __restrict__ wo,
                                                 const u16* __restrict__ gw, char* __restrict__ ws) {
    extern __shared__ __attribute__((aligned(16))) char smem[];
    LAS char* lds = (LAS char*)smem;
    const int tid = threadIdx.x, lane = tid & 63;
    const int wid = __builtin_amdgcn_readfirstlane(tid >> 6);
    const int bid = blockIdx.x;
    const int xj = bid & 7, mt = bid >> 3;
    const int r = lane & 31, h = lane >> 5;
    const int mi = wid & 1, ni = wid >> 1;
    const int s0 = mt & 7;
    const unsigned lds0 = (unsigned)(uintptr_t)lds;
    STAMP(ws, 0, 0);

    const int arow = mi * 32 + r, asw = (arow >> 1) & 7;
    int aoff[4];
#pragma unroll
    for (int ks = 0; ks < 4; ++ks) aoff[ks] = arow * 128 + (((2 * ks + h) ^ asw) << 4);
    const int g16 = lane >> 4, q = (lane & 15) >> 2, p = lane & 3;
    const int boff = K1_SA + (ni >> 1) * 8192 + (8 * (g16 >> 1) + q) * 128 + (((ni & 1) ^ (q >> 1)) << 6) + (g16 & 1) * 32 + p * 8;
    const int tt = mt * 2 + mi;
    f32x16 acc = (f32x16){};
    K1Frag f0, f1;

    if (wid < 8) {
        const int row = wid * 8 + (lane >> 3);
        const char* abase = (const char*)(x + (size_t)(mt * 64) * D_);
        const char* bbase = (const char*)(wqkv + 64 * xj);
        const unsigned ao = (row * D_ + (((lane & 7) ^ ((row >> 1) & 7)) << 3)) * 2;
        const unsigned bo = (row * (3 * D_) + (((lane & 7) ^ (((row >> 1) & 1) << 2)) << 3)) * 2;
        const unsigned adst = lds0 + wid * 1024, bdst = lds0 + K1_SA + wid * 1024;
        const bool storex = (mt >> 2) == xj;
        char* xf = ws + WS_XF + ((size_t)tt * NKS_D + ni) * 1024 + lane * 16;
#define K1_ISSUE(it, piece) do { const int st_ = ((it) + s0) & 7; const int bf_ = ((it) % K1_NBUF) * K1_ST; \
            if ((piece) == 0) glds16(abase + st_ * 128, ao, adst + bf_); \
            else glds16(bbase + (size_t)st_ * (128 * 3 * D_) + ((piece) - 1) * (2 * D_), bo, bdst + bf_ + ((piece) - 1) * 8192); } while (0)
#pragma unroll
        for (int it = 0; it < 3; ++it) { K1_ISSUE(it, 0); K1_ISSUE(it, 1); K1_ISSUE(it, 2); K1_ISSUE(it, 3); }
        WAIT_V(8);
        BAR();
        STAMP(ws, 0, 3);
        k1_ldfrag(f0, lds, aoff, boff);
#define K1_STAGE(FC, FN, it) do { \
            if ((it) < 7) { if ((it) < 6) WAIT_V(4); else WAIT_V(0); BAR(); k1_ldfrag(FN, lds + (((it) + 1) % K1_NBUF) * K1_ST, aoff, boff); } \
            A_MFMA_ACC(acc, FC.b[0], FC.a[0]); if ((it) + 3 < 8) K1_ISSUE((it) + 3, 0); \
            A_MFMA_ACC(acc, FC.b[1], FC.a[1]); if ((it) + 3 < 8) K1_ISSUE((it) + 3, 1); \
            A_MFMA_ACC(acc, FC.b[2], FC.a[2]); if ((it) + 3 < 8) K1_ISSUE((it) + 3, 2); \
            A_MFMA_ACC(acc, FC.b[3], FC.a[3]); if ((it) + 3 < 8) K1_ISSUE((it) + 3, 3); \
            if (storex) { const bf16x8 xv = ni == 0 ? FC.a[0] : ni == 1 ? FC.a[1] : ni == 2 ? FC.a[2] : FC.a[3]; \
                          *(bf16x8*)(xf + (size_t)(((it) + s0) & 7) * 4096) = xv; } \
        } while (0)
        K1_STAGE(f0, f1, 0); K1_STAGE(f1, f0, 1); K1_STAGE(f0, f1, 2); K1_STAGE(f1, f0, 3);
        K1_STAGE(f0, f1, 4); K1_STAGE(f1, f0, 5); K1_STAGE(f0, f1, 6); K1_STAGE(f1, f0, 7);
#undef K1_STAGE
#undef K1_ISSUE
        asm volatile("s_nop 7\n\ts_nop 7\n\ts_nop 3" : "+v"(acc));
        STAMP(ws, 0, 1);
        const int ix = 2 * xj + (ni & 1);
        if (ni < 2) acc = acc * (0.25f * 1.4426950408889634f);
        char* dst = ws + (ni < 2 ? WS_Q : WS_K);
#pragma unroll
        for (int s = 0; s < 2; ++s)
            *(u32x4*)(dst + ((size_t)(2 * ix + s) * 64 + tt) * 1024 + lane * 16) = pack8(acc, s);
    } else {
        const int sw = bid * 4 + (wid - 8);
        const unsigned scr = lds0 + K1_SCR + (wid - 8) * 4096;
#pragma unroll
        for (int i = 0; i < 4; ++i) {
            int f = sw + 1024 * i; if (f >= NFRAG) f = 0;
            const u16* wsrc; int ld = D_;
            if (f < 16 * NKS_F) wsrc = w2 + (size_t)(16 * (f % NKS_F)) * D_ + (f / NKS_F) * 32;
            else if (f < 16 * NKS_F + 16 * NKS_D) { f -= 16 * NKS_F; wsrc = wo + (size_t)(16 * (f & 31)) * D_ + (f >> 5) * 32; }
            else { f -= 16 * NKS_F + 16 * NKS_D; wsrc = w1 + (size_t)(16 * (f & 31)) * F_ + (f >> 5) * 32; ld = F_; }
            glds16((const char*)wsrc, ((lane >> 2) * ld + (lane & 3) * 8) * 2, scr + i * 1024);
        }
        u32x4 gx0, gx1, gg;
        asm volatile("global_load_dwordx4 %0, %1, %2" : "=v"(gx0) : "v"(lane * 16), "s"(x + (size_t)(2 * sw) * D_) : "memory");
        asm volatile("global_load_dwordx4 %0, %1, %2 offset:1024" : "=v"(gx1) : "v"(lane * 16), "s"(x + (size_t)(2 * sw) * D_) : "memory");
        asm volatile("global_load_dwordx4 %0, %1, %2" : "=v"(gg) : "v"(lane * 16), "s"(gw) : "memory");
        if (bid == 0 && wid == 8 && lane < 32) ((unsigned*)(ws + WS_CNT))[lane * 64] = 0u;
        BAR();
        k1_ldfrag(f0, lds, aoff, boff);
#define K1_STAGE(FC, FN, it) do { \
            if ((it) < 7) { BAR(); k1_ldfrag(FN, lds + (((it) + 1) % K1_NBUF) * K1_ST, aoff, boff); } \
            A_MFMA_ACC(acc, FC.a[0], FC.b[0]); A_MFMA_ACC(acc, FC.a[1], FC.b[1]); \
            A_MFMA_ACC(acc, FC.a[2], FC.b[2]); A_MFMA_ACC(acc, FC.a[3], FC.b[3]); \
        } while (0)
        K1_STAGE(f0, f1, 0); K1_STAGE(f1, f0, 1); K1_STAGE(f0, f1, 2); K1_STAGE(f1, f0, 3);
        asm volatile("s_waitcnt vmcnt(0)" : "+v"(gx0), "+v"(gx1), "+v"(gg) :: "memory");
#pragma unroll
        for (int i = 0; i < 4; ++i) {
            const int f = sw + 1024 * i;
            const bool nat = f >= 16 * NKS_F + 16 * NKS_D;
            const LAS char* rb = lds + K1_SCR + (wid - 8) * 4096 + i * 1024 + ((nat ? 8 : 4) * (g16 >> 1) + q) * 64 + (g16 & 1) * 32 + p * 8;
            v4i16_t lo = __builtin_amdgcn_ds_read_tr16_b64_v4i16((LAS v4i16_t*)(rb));
            v4i16_t hi = __builtin_amdgcn_ds_read_tr16_b64_v4i16((LAS v4i16_t*)(rb + (nat ? 256 : 512)));
            v8i16_t w = __builtin_shufflevector(lo, hi, 0, 1, 2, 3, 4, 5, 6, 7);
            if (f < NFRAG) *(v8i16_t*)(ws + WS_W2 + (size_t)f * 1024 + lane * 16) = w;
        }
        {
            float s0_ = 0.f, s1_ = 0.f;
#pragma unroll
            for (int j = 0; j < 4; ++j) {
                const float gl = __uint_as_float(gg[j] << 16), gh = __uint_as_float(gg[j] & 0xffff0000u);
                s0_ += __uint_as_float(gx0[j] << 16) * gl + __uint_as_float(gx0[j] & 0xffff0000u) * gh;
                s1_ += __uint_as_float(gx1[j] << 16) * gl + __uint_as_float(gx1[j] & 0xffff0000u) * gh;
            }
            float sv = h ? s1_ : s0_, so = h ? s0_ : s1_;
            sv += __shfl_xor(so, 32);
#pragma unroll
            for (int o = 16; o > 0; o >>= 1) sv += __shfl_xor(sv, o);
            if (r == 0) ((float*)(ws + WS_G))[2 * sw + h] = 1.f / (1.f + __expf(-sv));
        }
        K1_STAGE(f0, f1, 4); K1_STAGE(f1, f0, 5); K1_STAGE(f0, f1, 6); K1_STAGE(f1, f0, 7);
#undef K1_STAGE
        asm volatile("s_nop 7\n\ts_nop 7\n\ts_nop 3" : "+v"(acc));
        STAMP(ws, 0, 1);
        const int head = 2 * (2 * xj + (ni & 1)) + (r >> 4), d = r & 15;
#pragma unroll
        for (int s = 0; s < 2; ++s)
            *(u32x4*)(ws + WS_V + ((size_t)head * 128 + 2 * tt + s) * 512 + h * 256 + d * 16) = pack8(acc, s);
    }
    STAMP(ws, 0, 2);
}

#define X_NOLDS 0
#define X_NOMLP 0
constexpr int K2_CONST = 128 * 1024;
constexpr int K2_RED = K2_CONST + 20 * 512;
constexpr int K2_FLAG = K2_RED + 16384;
constexpr int K2_LDS = K2_FLAG + 32;
constexpr float K2_LIM = 65536.f;
constexpr int K2_PF = 8;

#define A_MFMA_S(D, K) asm volatile("v_mfma_f32_32x32x16_bf16 %0, %1, %2, %3" : "=&v"(D) : "v"(K), "v"(qf), "v"(negm) : "memory")
#define A_MFMA_PV(V, P) asm volatile("v_mfma_f32_32x32x16_bf16 %0, %1, %2, %0" : "+v"(og) : "v"(V), "v"(P))
#define A_MFMA_PV0(V, P) asm volatile("v_mfma_f32_32x32x16_bf16 %0, %1, %2, 0" : "=&v"(og) : "v"(V), "v"(P))
#define A_EXP4(E, S, i) asm volatile("v_exp_f32 %0, %4\n\tv_exp_f32 %1, %5\n\tv_exp_f32 %2, %6\n\tv_exp_f32 %3, %7" \
        : "=&v"(E[i]), "=&v"(E[i + 1]), "=&v"(E[i + 2]), "=&v"(E[i + 3]) : "v"(S[i]), "v"(S[i + 1]), "v"(S[i + 2]), "v"(S[i + 3]))
#define A_CVT4(P, E, i) asm volatile("v_cvt_pk_bf16_f32 %0, %4, %5\n\tv_cvt_pk_bf16_f32 %1, %6, %7\n\tv_cvt_pk_bf16_f32 %2, %8, %9\n\tv_cvt_pk_bf16_f32 %3, %10, %11" \
        : "=&v"(P[0]), "=&v"(P[1]), "=&v"(P[2]), "=&v"(P[3]) \
        : "v"(E[i]), "v"(E[i + 1]), "v"(E[i + 2]), "v"(E[i + 3]), "v"(E[i + 4]), "v"(E[i + 5]), "v"(E[i + 6]), "v"(E[i + 7]))

__global__ __launch_bounds__(768) void k2_kernel(char* __restrict__ ws, const u16* __restrict__ b1) {
    extern __shared__ __attribute__((aligned(16))) char smem[];
    LAS char* lds = (LAS char*)smem;
    const int tid = threadIdx.x, lane = tid & 63;
    const int wid = __builtin_amdgcn_readfirstlane(tid >> 6);
    const int bid = blockIdx.x;
    const int r = lane & 31, h = lane >> 5;
    STAMP(ws, 1, 0);

    if (tid < 2) { *(LAS u32x4*)(lds + K2_FLAG + tid * 16) = (u32x4){0u, 0u, 0u, 0u}; }
    asm volatile("s_waitcnt lgkmcnt(0)" ::: "memory");
    BAR();

    if (wid >= 8) {
        const int tb = (bid & 7) * 4 + ((bid >> 3) & 3), nb = bid >> 5;
        const int ni = wid & 1, kh = (wid >> 1) & 1;
        __builtin_amdgcn_s_setprio(3);
        const unsigned vo = lane * 16;
        volatile LAS unsigned* lw = (volatile LAS unsigned*)(lds + K2_FLAG);
        while (lw[2] < 8u) __builtin_amdgcn_s_sleep(4);
#define M_LOAD(D, sb) asm volatile("global_load_dwordx4 %0, %1, %2" : "=v"(D) : "v"(vo), "s"(sb) : "memory")
#define M_LOAD_SC1(D, sb) asm volatile("global_load_dwordx4 %0, %1, %2 sc1" : "=v"(D) : "v"(vo), "s"(sb) : "memory")
#define M_GAP() asm volatile("s_nop 7\n\ts_nop 3")
#define M_MFMA0(C, A, B) asm volatile("v_mfma_f32_32x32x16_bf16 %0, %1, %2, 0" : "=&v"(C) : "v"(A), "v"(B))
        const char* wf = ws + WS_W2 + ((size_t)(nb * 2 + ni) * NKS_F + kh * 43) * 1024;
        const char* af = ws + WS_ACT + ((size_t)(tb * 2) * NKS_F + kh * 43) * 1024;
        const int k2o = (nb * 5 + (tb & 3) * 11) % 43;
        bf16x8 fw[K2_PF], fa0[K2_PF], fa1[K2_PF];
        {
            const int wv = nb * 4 + (wid & 3), j = wv & 1, slot = wv >> 1;
            const int ft0 = slot < 11 ? 3 * slot : 33 + 2 * (slot - 11);
            const int ft2 = slot < 11 ? ft0 + 2 : ft0 + 1;
            const char* xb = ws + WS_XF + (size_t)(tb * 2 + j) * (NKS_D * 1024);
            const char* wb0 = ws + WS_W1 + (size_t)ft0 * (NKS_D * 1024);
            const char* wb2 = ws + WS_W1 + (size_t)ft2 * (NKS_D * 1024);
            const int k1o = ((tb & 3) * 2 + j) * 4;
            {
                const size_t o = (size_t)(((bid >> 3) * 4 + (wid & 3)) * 4) * 1024 + lane * 16;
                u32x4 cp[4];
#pragma unroll
                for (int i = 0; i < 4; ++i) cp[i] = *(const u32x4*)(ws + WS_WO + o + i * 1024);
#pragma unroll
                for (int i = 0; i < 4; ++i) *(u32x4*)(ws + WS_WOX + (size_t)(bid & 7) * (16 * NKS_D * 1024) + o + i * 1024) = cp[i];
                asm volatile("s_waitcnt vmcnt(0)" ::: "memory");
            }
            bf16x8 fx[6], f0[6], f1[6], f2[6];
            int kq1 = k1o;
#define P1_ISSUE(i) do { const int kk_ = kq1 * 1024; kq1 = (kq1 + 1) & 31; \
                M_LOAD(fx[i], xb + kk_); M_LOAD(f0[i], wb0 + kk_); M_LOAD(f1[i], wb0 + NKS_D * 1024 + kk_); M_LOAD(f2[i], wb2 + kk_); } while (0)
#define P1_STEP(i, n) do { asm volatile("s_waitcnt vmcnt(" #n ")" : "+v"(fx[i]), "+v"(f0[i]), "+v"(f1[i]), "+v"(f2[i]) :: "memory"); \
                A_MFMA_ACC(c[0], f0[i], fx[i]); M_GAP(); A_MFMA_ACC(c[1], f1[i], fx[i]); M_GAP(); A_MFMA_ACC(c[2], f2[i], fx[i]); } while (0)
            P1_ISSUE(0); P1_ISSUE(1); P1_ISSUE(2); P1_ISSUE(3); P1_ISSUE(4); P1_ISSUE(5);
            f32x16 c[3];
#pragma unroll
            for (int t = 0; t < 3; ++t) {
                const unsigned* bs = (const unsigned*)(b1 + 32 * (t < 2 ? ft0 + t : ft2));
#pragma unroll
                for (int e = 0; e < 8; ++e) {
                    const int d0 = (e & 1) + 4 * (e >> 1);
                    const unsigned bw = h ? bs[d0 + 2] : bs[d0];
                    c[t][2 * e] = __uint_as_float(bw << 16); c[t][2 * e + 1] = __uint_as_float(bw & 0xffff0000u);
                }
            }
            asm volatile("s_nop 1" : "+v"(c[0]), "+v"(c[1]), "+v"(c[2]));
            for (int rd = 0; rd < 4; ++rd) {
                P1_STEP(0, 20); P1_ISSUE(0); P1_STEP(1, 20); P1_ISSUE(1); P1_STEP(2, 20); P1_ISSUE(2);
                P1_STEP(3, 20); P1_ISSUE(3); P1_STEP(4, 20); P1_ISSUE(4); P1_STEP(5, 20); P1_ISSUE(5);
            }
            P1_STEP(0, 20); P1_ISSUE(0); P1_STEP(1, 20); P1_ISSUE(1);
            P1_STEP(2, 20); P1_STEP(3, 16); P1_STEP(4, 12); P1_STEP(5, 8); P1_STEP(0, 4); P1_STEP(1, 0);
#undef P1_ISSUE
#undef P1_STEP
            {
                int kk = k2o;
#pragma unroll
                for (int k = 0; k < K2_PF; ++k) { M_LOAD(fw[k], wf + kk * 1024); kk = kk == 42 ? 0 : kk + 1; }
            }
            asm volatile("s_nop 7\n\ts_nop 7\n\ts_nop 3" : "+v"(c[0]), "+v"(c[1]), "+v"(c[2]));
            STAMP(ws, 1, 4);
#pragma unroll
            for (int t = 0; t < 3; ++t) {
                const int ft = t < 2 ? ft0 + t : ft2;
                f32x16 a = c[t];
#pragma unroll
                for (int e = 0; e < 16; ++e) a[e] = mish_f(a[e]);
                const char* dst = ws + WS_ACT + ((size_t)(tb * 2 + j) * NKS_F + 2 * ft) * 1024;
                const u32x4 p0 = pack8(a, 0), p1 = pack8(a, 1);
                asm volatile("global_store_dwordx4 %0, %1, %2 sc1\n\ts_nop 1" :: "v"(vo), "v"(p0), "s"(dst) : "memory");
                asm volatile("global_store_dwordx4 %0, %1, %2 offset:1024 sc1\n\ts_nop 1" :: "v"(vo), "v"(p1), "s"(dst) : "memory");
            }
            asm volatile("s_waitcnt vmcnt(0)" : "+v"(fw[0]), "+v"(fw[1]), "+v"(fw[2]), "+v"(fw[3]), "+v"(fw[4]), "+v"(fw[5]), "+v"(fw[6]), "+v"(fw[7]) :: "memory");
            STAMP(ws, 1, 5);
            unsigned old = 0;
            if (lane == 0) old = __hip_atomic_fetch_add((LAS unsigned*)(lw + 4), 1u, __ATOMIC_RELAXED, __HIP_MEMORY_SCOPE_WORKGROUP);
            if (__builtin_amdgcn_readfirstlane(old) == 3u) {
                GAS unsigned* cnt = (GAS unsigned*)(ws + WS_CNT) + tb * 64;
                if (lane == 0) __hip_atomic_fetch_add(cnt, 1u, __ATOMIC_RELAXED, __HIP_MEMORY_SCOPE_AGENT);
                for (unsigned spins = 0; spins < (1u << 22); ++spins) {
                    if ((unsigned)__builtin_amdgcn_readfirstlane(__hip_atomic_load(cnt, __ATOMIC_RELAXED, __HIP_MEMORY_SCOPE_AGENT)) >= 8u) break;
                    __builtin_amdgcn_s_sleep(4);
                }
                asm volatile("s_waitcnt vmcnt(0)" ::: "memory");
                if (lane == 0) lw[5] = 1u;
            } else {
                while (lw[5] == 0u) __builtin_amdgcn_s_sleep(2);
            }
            asm volatile("s_waitcnt vmcnt(0) lgkmcnt(0)" ::: "memory");
            STAMP(ws, 1, 6);
        }
        STAMP(ws, 1, 1);
        f32x16 c0, c1;
        int kq2 = k2o;
#pragma unroll
        for (int k = 0; k < K2_PF; ++k) { M_LOAD_SC1(fa0[k], af + kq2 * 1024); M_LOAD_SC1(fa1[k], af + (NKS_F + kq2) * 1024); kq2 = kq2 == 42 ? 0 : kq2 + 1; }
#define P2_ISSUE(i) do { const int kk_ = kq2 * 1024; kq2 = kq2 == 42 ? 0 : kq2 + 1; \
            M_LOAD(fw[i], wf + kk_); M_LOAD_SC1(fa0[i], af + kk_); M_LOAD_SC1(fa1[i], af + NKS_F * 1024 + kk_); } while (0)
#define P2_WAIT(i, n) asm volatile("s_waitcnt vmcnt(" #n ")" : "+v"(fw[i]), "+v"(fa0[i]), "+v"(fa1[i]) :: "memory")
#define P2_STEP(i, n) do { P2_WAIT(i, n); A_MFMA_ACC(c0, fw[i], fa0[i]); M_GAP(); A_MFMA_ACC(c1, fw[i], fa1[i]); } while (0)
        P2_WAIT(0, 14); M_MFMA0(c0, fw[0], fa0[0]); M_MFMA0(c1, fw[0], fa1[0]); P2_ISSUE(0);
        P2_STEP(1, 15); P2_ISSUE(1); P2_STEP(2, 16); P2_ISSUE(2); P2_STEP(3, 17); P2_ISSUE(3);
        P2_STEP(4, 18); P2_ISSUE(4); P2_STEP(5, 19); P2_ISSUE(5); P2_STEP(6, 20); P2_ISSUE(6); P2_STEP(7, 21); P2_ISSUE(7);
        for (int rd = 0; rd < 3; ++rd) {
            P2_STEP(0, 21); P2_ISSUE(0); P2_STEP(1, 21); P2_ISSUE(1); P2_STEP(2, 21); P2_ISSUE(2); P2_STEP(3, 21); P2_ISSUE(3);
            P2_STEP(4, 21); P2_ISSUE(4); P2_STEP(5, 21); P2_ISSUE(5); P2_STEP(6, 21); P2_ISSUE(6); P2_STEP(7, 21); P2_ISSUE(7);
        }
        P2_STEP(0, 21); P2_ISSUE(0); P2_STEP(1, 21); P2_ISSUE(1); P2_STEP(2, 21); P2_ISSUE(2);
        P2_STEP(3, 21); P2_STEP(4, 18); P2_STEP(5, 15); P2_STEP(6, 12); P2_STEP(7, 9); P2_STEP(0, 6); P2_STEP(1, 3); P2_STEP(2, 0);
#undef P2_ISSUE
#undef P2_WAIT
#undef P2_STEP
        asm volatile("s_nop 7\n\ts_nop 7\n\ts_nop 3" : "+v"(c0), "+v"(c1));
        LAS f32x4* red = (LAS f32x4*)(lds + K2_RED + ni * 8192);
        volatile LAS unsigned* flag = (volatile LAS unsigned*)(lds + K2_FLAG) + ni;
        if (kh == 1) {
#pragma unroll
            for (int qd = 0; qd < 4; ++qd) {
                red[qd * 64 + lane] = (f32x4){c0[4 * qd], c0[4 * qd + 1], c0[4 * qd + 2], c0[4 * qd + 3]};
                red[(4 + qd) * 64 + lane] = (f32x4){c1[4 * qd], c1[4 * qd + 1], c1[4 * qd + 2], c1[4 * qd + 3]};
            }
            asm volatile("s_waitcnt lgkmcnt(0)" ::: "memory");
            if (lane == 0) *flag = 1u;
        } else {
            const float* gp = (const float*)(ws + WS_G) + tb * 64;
            const float og0 = 1.f - gp[r], og1 = 1.f - gp[32 + r];
            while (*flag == 0u) __builtin_amdgcn_s_sleep(2);
            asm volatile("" ::: "memory");
            f32x4* dst = (f32x4*)(ws + WS_B) + ((size_t)(tb * 8 + nb) * 16 + ni * 8) * 64 + lane;
#pragma unroll
            for (int qd = 0; qd < 4; ++qd) {
                f32x4 u0 = red[qd * 64 + lane], u1 = red[(4 + qd) * 64 + lane];
                u0 += (f32x4){c0[4 * qd], c0[4 * qd + 1], c0[4 * qd + 2], c0[4 * qd + 3]};
                u1 += (f32x4){c1[4 * qd], c1[4 * qd + 1], c1[4 * qd + 2], c1[4 * qd + 3]};
                dst[qd * 64] = u0 * og0;
                dst[(4 + qd) * 64] = u1 * og1;
            }
        }
        STAMP(ws, 1, 3);
        return;
    }

    const int head = (bid & 7) * 4 + ((bid >> 3) & 3), qb = bid >> 5;
    const int qt = qb * 8 + wid;
    bf16x8 qf;
    asm volatile("global_load_dwordx4 %0, %1, %2" : "=v"(qf) : "v"(lane * 16), "s"(ws + WS_Q + ((size_t)head * 64 + qt) * 1024) : "memory");
    {
        const char* kg = ws + WS_K + (size_t)head * 65536 + wid * 1024;
        const char* vg = ws + WS_V + (size_t)head * 65536 + wid * 1024;
        const unsigned lds0 = (unsigned)(uintptr_t)lds + wid * 1024;
#pragma unroll
        for (int i = 0; i < 8; ++i) {
            glds16(kg + i * 8192, lane * 16, lds0 + i * 8192);
            glds16(vg + i * 8192, lane * 16, lds0 + 65536 + i * 8192);
        }
    }
    if (tid < 40) {
        const unsigned c = (tid & 1) ? 0u : 0x3f803f80u;
        *(LAS u32x4*)(lds + K2_CONST + (tid >> 1) * 512 + (tid & 1) * 16) = (u32x4){c, c, c, c};
    }
    const bool vact = r < 16;
    const LAS char* kp = lds + lane * 16;
    const LAS char* vp = vact ? lds + 65536 + h * 256 + r * 16 : lds + K2_CONST + (r == 16 ? 0 : 16);
    const int vinc = vact ? 8192 : 0;

    f32x16 ot = (f32x16){};
    f32x16 negm, sa, sb;
    bf16x8 ka, kb, va0, va1, vb0, vb1;
    if (X_NOLDS) { vb0 = *(const LAS bf16x8*)(vp); vb1 = *(const LAS bf16x8*)(vp + 512); }
    float m;
#define K2_SYNC(c) do { volatile LAS unsigned* c_ = (volatile LAS unsigned*)(lds + K2_FLAG) + 2 + (c); \
        if (lane == 0) __hip_atomic_fetch_add((LAS unsigned*)c_, 1u, __ATOMIC_RELAXED, __HIP_MEMORY_SCOPE_WORKGROUP); \
        while (*c_ < 8u) __builtin_amdgcn_s_sleep(1); \
        asm volatile("" ::: "memory"); } while (0)
    asm volatile("s_waitcnt vmcnt(12) lgkmcnt(0)" : "+v"(qf) :: "memory");
    K2_SYNC(0);
    STAMP(ws, 1, 1);
    {
        ka = *(const LAS bf16x8*)kp;
        kb = *(const LAS bf16x8*)(kp + 1024);
        va0 = *(const LAS bf16x8*)(vp); va1 = *(const LAS bf16x8*)(vp + 512);
        f32x16 s = MFMA(ka, qf, (f32x16){});
        float mx = fmaxf(s[0], s[1]);
#pragma unroll
        for (int e = 2; e < 16; ++e) mx = fmaxf(mx, s[e]);
        mx = fmaxf(mx, __shfl_xor(mx, 32));
        m = mx;
#pragma unroll
        for (int e = 0; e < 16; ++e) negm[e] = -m;
        asm volatile("s_nop 3" : "+v"(negm));
        A_MFMA_S(sa, ka);
        asm volatile("s_nop 7\n\ts_nop 7" : "+v"(sa));
    }
    const LAS char* kq = kp;
    const LAS char* vq = vp;
    int g = 0;
    for (; g < 8; ++g) {
        if (g == 1) { WAIT_V(0); K2_SYNC(1); }
        if (g == 0) __builtin_amdgcn_s_setprio(2);
        else if (g == 5) __builtin_amdgcn_s_setprio(1);
        else if (g == 7) __builtin_amdgcn_s_setprio(0);
        const LAS char* kq2 = (g == 7) ? kp - 8192 : kq;
        const LAS char* vq2 = (g == 7) ? vp - vinc : vq;
        asm volatile("" : "+v"(kq), "+v"(vq), "+v"(kq2), "+v"(vq2));
        f32x16 og;
        float e[16];
        u32x4 p0, p1;
#define K2_TILE(SC, SN, KC, KN, VC0, VP1, VN0, VN1, t) do { \
            if ((t) > 0) A_MFMA_PV(VP1, p1); \
            if (!X_NOLDS) { \
            KN = ((t) + 2 < 8) ? *(const LAS bf16x8*)(kq + ((t) + 2) * 1024) : *(const LAS bf16x8*)(kq2 + ((t) + 2) * 1024); \
            if ((t) + 1 < 8) { VN0 = *(const LAS bf16x8*)(vq + ((t) + 1) * 1024); VN1 = *(const LAS bf16x8*)(vq + ((t) + 1) * 1024 + 512); } \
            else { VN0 = *(const LAS bf16x8*)(vq2 + ((t) + 1) * 1024); VN1 = *(const LAS bf16x8*)(vq2 + ((t) + 1) * 1024 + 512); } } \
            A_EXP4(e, SC, 0); A_EXP4(e, SC, 4); \
            A_MFMA_S(SN, KC); \
            A_CVT4(p0, e, 0); \
            A_EXP4(e, SC, 8); \
            if ((t) == 0) A_MFMA_PV0(VC0, p0); else A_MFMA_PV(VC0, p0); \
            A_EXP4(e, SC, 12); \
            A_CVT4(p1, e, 8); \
            asm volatile("s_nop 1"); \
        } while (0)
#pragma unroll
        for (int t = 0; t < 8; t += 2) {
            K2_TILE(sa, sb, kb, ka, va0, vb1, vb0, vb1, t);
            K2_TILE(sb, sa, ka, kb, vb0, va1, va0, va1, t + 1);
        }
        A_MFMA_PV(vb1, p1);
        asm volatile("s_nop 7\n\ts_nop 7\n\ts_nop 3" : "+v"(og));
#undef K2_TILE
        if (__builtin_expect(__any(!(og[8] <= K2_LIM)), 0)) break;
#pragma unroll
        for (int i = 0; i < 9; ++i) ot[i] += og[i];
        kq += 8192; vq += vinc;
    }
    if (__builtin_expect(g < 8, 0)) {
        if (g == 0) { WAIT_V(0); K2_SYNC(1); }
        for (int t = g * 8; t < 64; ++t) {
            bf16x8 kf = *(const LAS bf16x8*)(kp + t * 1024);
            const LAS char* vt = vp + (vact ? t * 1024 : 0);
            vb0 = *(const LAS bf16x8*)(vt); vb1 = *(const LAS bf16x8*)(vt + 512);
            f32x16 s = MFMA(kf, qf, (f32x16){});
            float mx = fmaxf(s[0], s[1]);
#pragma unroll
            for (int i = 2; i < 16; ++i) mx = fmaxf(mx, s[i]);
            mx = fmaxf(mx, __shfl_xor(mx, 32));
            float mn = fmaxf(m, mx);
            float al = __builtin_amdgcn_exp2f(m - mn);
            m = mn;
            ot = ot * al;
#pragma unroll
            for (int i = 0; i < 16; ++i) s[i] = __builtin_amdgcn_exp2f(s[i] - m);
            ot = MFMA(vb0, __builtin_bit_cast(bf16x8, pack8(s, 0)), ot);
            ot = MFMA(vb1, __builtin_bit_cast(bf16x8, pack8(s, 1)), ot);
        }
    }
    STAMP(ws, 1, 2);
    float l = __shfl(ot[8], r);
    float rl = 1.f / l;
    f32x16 on = ot * rl;
    *(u32x4*)(ws + WS_O + ((size_t)qt * NKS_D + head) * 1024 + lane * 16) = pack8(on, 0);
    STAMP(ws, 1, 3);
}

constexpr int K3_LDS = 64 * 1024;

#define A_GLOAD(D, voff, sbase, imm) asm volatile("global_load_dwordx4 %0, %1, %2 offset:" #imm : "=v"(D) : "v"(voff), "s"(sbase) : "memory")

__global__ __launch_bounds__(256) void k3_kernel(const char* __restrict__ ws, u16* __restrict__ out) {
    extern __shared__ __attribute__((aligned(16))) char smem[];
    const int tid = threadIdx.x, lane = tid & 63;
    const int wid = __builtin_amdgcn_readfirstlane(tid >> 6);
    const int bid = blockIdx.x;
    const int tt = (bid & 7) * 8 + ((bid >> 3) & 7), nq = bid >> 6;
    const int r = lane & 31, h = lane >> 5;
    STAMP(ws, 2, 0);

    const char* af = ws + WS_O + ((size_t)tt * NKS_D + wid * 8) * 1024;
    const char* wf = ws + WS_WOX + (size_t)(bid & 7) * (16 * NKS_D * 1024) + ((size_t)(nq * 4) * NKS_D + wid * 8) * 1024;
    const unsigned vo = lane * 16, vo4 = lane * 16 + 4096;
    bf16x8 a[8], w[4][8];
    A_GLOAD(a[0], vo, af, 0); A_GLOAD(a[1], vo, af, 1024); A_GLOAD(a[2], vo, af, 2048); A_GLOAD(a[3], vo, af, 3072);
    A_GLOAD(a[4], vo4, af, 0); A_GLOAD(a[5], vo4, af, 1024); A_GLOAD(a[6], vo4, af, 2048); A_GLOAD(a[7], vo4, af, 3072);
#pragma unroll
    for (int t = 0; t < 4; ++t) {
        const char* wt = wf + (size_t)t * NKS_D * 1024;
        A_GLOAD(w[t][0], vo, wt, 0); A_GLOAD(w[t][1], vo, wt, 1024); A_GLOAD(w[t][2], vo, wt, 2048); A_GLOAD(w[t][3], vo, wt, 3072);
        A_GLOAD(w[t][4], vo4, wt, 0); A_GLOAD(w[t][5], vo4, wt, 1024); A_GLOAD(w[t][6], vo4, wt, 2048); A_GLOAD(w[t][7], vo4, wt, 3072);
    }
    const int nt = nq * 4 + wid;
    const f32x4* bp = (const f32x4*)(ws + WS_B) + ((size_t)((tt >> 1) * 8 + (nt >> 1)) * 16 + ((nt & 1) * 2 + (tt & 1)) * 4) * 64 + lane;
    f32x4 bq[4];
#pragma unroll
    for (int qd = 0; qd < 4; ++qd) bq[qd] = bp[qd * 64];
    const float g = ((const float*)(ws + WS_G))[tt * 32 + r];

    f32x16 acc[4];
#pragma unroll
    for (int t = 0; t < 4; ++t) acc[t] = (f32x16){};
#define K3_W(t, n) asm volatile("s_waitcnt vmcnt(" #n ")" : "+v"(w[t][0]), "+v"(w[t][1]), "+v"(w[t][2]), "+v"(w[t][3]), "+v"(w[t][4]), "+v"(w[t][5]), "+v"(w[t][6]), "+v"(w[t][7]) :: "memory")
    asm volatile("s_waitcnt vmcnt(37)" : "+v"(a[0]), "+v"(a[1]), "+v"(a[2]), "+v"(a[3]), "+v"(a[4]), "+v"(a[5]), "+v"(a[6]), "+v"(a[7]) :: "memory");
    K3_W(0, 29);
#pragma unroll
    for (int i = 0; i < 8; ++i) acc[0] = MFMA(w[0][i], a[i], acc[0]);
    K3_W(1, 21);
#pragma unroll
    for (int i = 0; i < 8; ++i) acc[1] = MFMA(w[1][i], a[i], acc[1]);
    K3_W(2, 13);
#pragma unroll
    for (int i = 0; i < 8; ++i) acc[2] = MFMA(w[2][i], a[i], acc[2]);
    K3_W(3, 5);
#pragma unroll
    for (int i = 0; i < 8; ++i) acc[3] = MFMA(w[3][i], a[i], acc[3]);
#undef K3_W
    STAMP(ws, 2, 1);
    f32x4* red = (f32x4*)smem;
#pragma unroll
    for (int t = 0; t < 4; ++t)
#pragma unroll
        for (int qd = 0; qd < 4; ++qd)
            red[((wid * 4 + t) * 4 + qd) * 64 + lane] = (f32x4){acc[t][4 * qd], acc[t][4 * qd + 1], acc[t][4 * qd + 2], acc[t][4 * qd + 3]};
    __syncthreads();
    const int tok = tt * 32 + r;
#pragma unroll
    for (int qd = 0; qd < 4; ++qd) {
        f32x4 sacc = red[(wid * 4 + qd) * 64 + lane];
#pragma unroll
        for (int k = 1; k < 4; ++k) sacc += red[((k * 4 + wid) * 4 + qd) * 64 + lane];
        sacc = sacc * g + bq[qd];
        const int n = nt * 32 + 8 * qd + 4 * h;
        u32x2 o = {pk2(sacc[0], sacc[1]), pk2(sacc[2], sacc[3])};
        *(u32x2*)(out + (size_t)tok * D_ + n) = o;
    }
    STAMP(ws, 2, 2);
}

extern "C" void kernel_launch(void* const* d_in, const int* in_sizes, int n_in,
                              void* d_out, int out_size, void* d_ws, size_t ws_size,
                              hipStream_t stream) {
    const u16* x = (const u16*)d_in[0];
    const u16* wqkv = (const u16*)d_in[1];
    const u16* wo = (const u16*)d_in[2];
    const u16* w1 = (const u16*)d_in[4];
    const u16* b1 = (const u16*)d_in[5];
    const u16* w2 = (const u16*)d_in[6];
    const u16* gw = (const u16*)d_in[7];
    char* ws = (char*)d_ws;
    (void)hipFuncSetAttribute((const void*)k1_kernel, hipFuncAttributeMaxDynamicSharedMemorySize, K1_LDS);
    (void)hipFuncSetAttribute((const void*)k2_kernel, hipFuncAttributeMaxDynamicSharedMemorySize, K2_LDS);
    (void)hipFuncSetAttribute((const void*)k3_kernel, hipFuncAttributeMaxDynamicSharedMemorySize, K3_LDS);
    k1_kernel<<<256, 768, K1_LDS, stream>>>(x, wqkv, w1, w2, wo, gw, ws);
    k2_kernel<<<256, 768, K2_LDS, stream>>>(ws, b1);
    k3_kernel<<<256, 256, K3_LDS, stream>>>(ws, (u16*)d_out);
}
```

```cpp
#include <hip/hip_runtime.h>
#include <hip/hip_bf16.h>
#include <stdint.h>

typedef __attribute__((ext_vector_type(8))) __bf16 bf16x8;
typedef __attribute__((ext_vector_type(2))) __bf16 bf16x2;
typedef __attribute__((ext_vector_type(2))) float f32x2;
typedef __attribute__((ext_vector_type(4))) float f32x4;
typedef __attribute__((ext_vector_type(16))) float f32x16;
typedef __attribute__((ext_vector_type(4))) unsigned u32x4;
typedef __attribute__((ext_vector_type(2))) unsigned u32x2;
typedef short v4i16_t __attribute__((ext_vector_type(4)));
typedef short v8i16_t __attribute__((ext_vector_type(8)));
typedef unsigned short u16;
#define LAS __attribute__((address_space(3)))
#define GAS __attribute__((address_space(1)))
#define DI __device__ __forceinline__
#define MFMA(a, b, c) __builtin_amdgcn_mfma_f32_32x32x16_bf16((a), (b), (c), 0, 0, 0)

constexpr int S_ = 2048, D_ = 512, H_ = 32, F_ = 1376;
constexpr int NKS_F = F_ / 16;
constexpr int NKS_D = D_ / 16;

constexpr size_t WS_Q = 0;
constexpr size_t WS_K = WS_Q + (size_t)2 * 1024 * 1024;
constexpr size_t WS_V = WS_K + (size_t)2 * 1024 * 1024;
constexpr size_t WS_O = WS_V + (size_t)2 * 1024 * 1024;
constexpr size_t WS_ACT = WS_O + (size_t)2 * 1024 * 1024;
constexpr size_t WS_W2 = WS_ACT + (size_t)64 * NKS_F * 1024;
constexpr size_t WS_W1 = WS_W2 + (size_t)16 * NKS_F * 1024;
constexpr size_t WS_XF = WS_W1 + (size_t)43 * NKS_D * 1024;
constexpr size_t WS_CNT = WS_XF + (size_t)64 * NKS_D * 1024;
constexpr size_t WS_G = WS_CNT + 32 * 256;
constexpr size_t WS_WOX = WS_G + 8192;
constexpr size_t WS_B = WS_WOX + (size_t)8 * 16 * NKS_D * 1024;

DI unsigned pk2(float a, float b) { bf16x2 r = __builtin_convertvector((f32x2){a, b}, bf16x2); return __builtin_bit_cast(unsigned, r); }
DI float bf2f(u16 b) { return __uint_as_float((unsigned)b << 16); }
DI int crow(int r, int h) { return (r & 3) + 8 * (r >> 2) + 4 * h; }
DI u32x4 pack8(const f32x16& x, int s) {
    u32x4 p;
    p[0] = pk2(x[8 * s + 0], x[8 * s + 1]); p[1] = pk2(x[8 * s + 2], x[8 * s + 3]);
    p[2] = pk2(x[8 * s + 4], x[8 * s + 5]); p[3] = pk2(x[8 * s + 6], x[8 * s + 7]);
    return p;
}
DI void glds16(const char* sbase, unsigned voff, unsigned lds_base) {
    unsigned sv;
    asm volatile("s_mov_b32 %0, m0\n\ts_mov_b32 m0, %3\n\ts_nop 0\n\tglobal_load_lds_dwordx4 %1, %2\n\ts_mov_b32 m0, %0"
                 : "=&s"(sv) : "v"(voff), "s"(sbase), "s"(lds_base) : "memory");
}
#define STAMP(ws, kid, idx)
#define STAMPL(i)
#define STAMPL_FLUSH(ws, kid)
#define WAIT_V(n) asm volatile("s_waitcnt vmcnt(" #n ")" ::: "memory")
#define BAR() __builtin_amdgcn_s_barrier()

DI float mish_f(float h) {
    const float n = __builtin_amdgcn_exp2f(fminf(h, 20.f) * 1.4426950408889634f);
    const float w = n * (n + 2.f);
    return h * w * __builtin_amdgcn_rcpf(w + 2.f);
}

constexpr int K1_SA = 64 * 64 * 2;
constexpr int K1_SB = 3 * 64 * 64 * 2;
constexpr int K1_BOFF = 8 * K1_SA;
constexpr int K1_LDS = K1_BOFF + 4 * K1_SB;
constexpr int NFRAG = 16 * NKS_F + 43 * NKS_D;

#define A_MFMA_ACC(C, A, B) asm volatile("v_mfma_f32_32x32x16_bf16 %0, %1, %2, %0" : "+v"(C) : "v"(A), "v"(B))

struct K1Frag { bf16x8 a[4], b[4]; };
DI void k1_ldfrag(K1Frag& f, const LAS char* lds, int it, const int (&aoff)[4], int boff) {
    int ao_ = it * K1_SA, bo_ = (it & 3) * K1_SB;
    asm volatile("" : "+s"(ao_), "+s"(bo_));
#pragma unroll
    for (int ks = 0; ks < 4; ++ks) {
        f.a[ks] = *(const LAS bf16x8*)(lds + ao_ + aoff[ks]);
        v4i16_t lo = __builtin_amdgcn_ds_read_tr16_b64_v4i16((LAS v4i16_t*)(lds + bo_ + boff + ks * 2048));
        v4i16_t hi = __builtin_amdgcn_ds_read_tr16_b64_v4i16((LAS v4i16_t*)(lds + bo_ + boff + ks * 2048 + 512));
        v8i16_t w = __builtin_shufflevector(lo, hi, 0, 1, 2, 3, 4, 5, 6, 7);
        f.b[ks] = __builtin_bit_cast(bf16x8, w);
    }
}

__global__ __launch_bounds__(1024) void k1_kernel(const u16* __restrict__ x, const u16* __restrict__ wqkv, const u16* __restrict__ w1,
                                                  const u16* __restrict__ w2, char* __restrict__ ws) {
    extern __shared__ __attribute__((aligned(16))) char smem[];
    LAS char* lds = (LAS char*)smem;
    const int tid = threadIdx.x, lane = tid & 63;
    const int wid = __builtin_amdgcn_readfirstlane(tid >> 6);
    const int bid = blockIdx.x;
    const int xj = (bid & 6) + (bid >> 7), mt = (bid & 1) * 16 + ((bid >> 3) & 15);
    const int r = lane & 31, h = lane >> 5;
    const int s0 = mt & 7;
    const unsigned lds0 = (unsigned)(uintptr_t)lds;
    STAMP(ws, 0, 0);

    if (wid >= 12) {
        const int lw = wid - 12;
        const int row = lw * 16 + (lane >> 3);
        const char* abase = (const char*)(x + (size_t)(mt * 64) * D_);
        const char* bbase = (const char*)(wqkv + 64 * xj);
        const unsigned ao0 = (row * D_ + (((lane & 7) ^ ((row >> 1) & 7)) << 3)) * 2;
        const unsigned ao1 = ((row + 8) * D_ + (((lane & 7) ^ (((row + 8) >> 1) & 7)) << 3)) * 2;
        const unsigned bo0 = (row * (3 * D_) + (((lane & 7) ^ (((row >> 1) & 1) << 2)) << 3)) * 2;
        const unsigned bo1 = bo0 + 8 * (3 * D_) * 2;
        const unsigned adst = lds0 + lw * 2048, bdst = lds0 + K1_BOFF + lw * 2048;
#define K1_ISSUE_A(it) do { const int st_ = ((it) + s0) & 7; \
            glds16(abase + st_ * 128, ao0, adst + (it) * K1_SA); glds16(abase + st_ * 128, ao1, adst + (it) * K1_SA + 1024); } while (0)
#define K1_ISSUE_B(it) do { const int st_ = ((it) + s0) & 7; const int bf_ = ((it) & 3) * K1_SB; \
            const char* b_ = bbase + (size_t)st_ * (128 * 3 * D_); \
            _Pragma("unroll") for (int j_ = 0; j_ < 3; ++j_) { \
                glds16(b_ + j_ * (2 * D_), bo0, bdst + bf_ + j_ * 8192); glds16(b_ + j_ * (2 * D_), bo1, bdst + bf_ + j_ * 8192 + 1024); } } while (0)
        K1_ISSUE_A(0); K1_ISSUE_B(0); K1_ISSUE_A(1); K1_ISSUE_B(1);
        WAIT_V(8);  BAR(); K1_ISSUE_A(2); K1_ISSUE_B(2); K1_ISSUE_A(3); K1_ISSUE_B(3);
        WAIT_V(16); BAR(); K1_ISSUE_B(4); K1_ISSUE_A(4); K1_ISSUE_A(5);
        WAIT_V(18); BAR(); K1_ISSUE_B(5); K1_ISSUE_A(6); K1_ISSUE_A(7);
        WAIT_V(20); BAR(); K1_ISSUE_B(6);
        WAIT_V(18); BAR(); K1_ISSUE_B(7);
        WAIT_V(16); BAR();
        WAIT_V(6);  BAR();
        WAIT_V(0);  BAR();
#undef K1_ISSUE_A
#undef K1_ISSUE_B
        if (bid == 0 && lw == 0 && lane < 32) ((unsigned*)(ws + WS_CNT))[lane * 64] = 0u;
        return;
    }

    const int mi = wid & 1, ni = wid >> 1;
    const int tt = mt * 2 + mi;
    const int rf = bid * 12 + wid;
    const bool nat = rf >= 16 * NKS_F;
#define K1_ADDR() int ln = lane; asm volatile("" : "+v"(ln)); \
    const int arow = mi * 32 + (ln & 31), asw = (arow >> 1) & 7; \
    int aoff[4]; \
    _Pragma("unroll") for (int ks = 0; ks < 4; ++ks) aoff[ks] = arow * 128 + (((2 * ks + (ln >> 5)) ^ asw) << 4); \
    const int boff = K1_BOFF + (ni >> 1) * 8192 + (8 * (ln >> 5) + ((ln & 15) >> 2)) * 128 + (((ni & 1) ^ ((ln & 15) >> 3)) << 6) + ((ln >> 4) & 1) * 32 + (ln & 3) * 8;
#define K1_RLOAD() do { int f = rf < NFRAG ? rf : 0; const u16* wsrc; int ld; \
        if (f < 16 * NKS_F) { wsrc = w2 + (size_t)(16 * (f % NKS_F)) * D_ + (f / NKS_F) * 32; ld = D_; } \
        else { f -= 16 * NKS_F; wsrc = w1 + (size_t)(16 * (f & 31)) * F_ + (f >> 5) * 32; ld = F_; } \
        glds16((const char*)wsrc, ((ln >> 2) * ld + (ln & 3) * 8) * 2, lds0 + wid * 1024); } while (0)
#define K1_REPACK() do { asm volatile("s_waitcnt vmcnt(0)" ::: "memory"); \
        const LAS char* rb = lds + wid * 1024 + ((nat ? 8 : 4) * (ln >> 5) + ((ln & 15) >> 2)) * 64 + ((ln >> 4) & 1) * 32 + (ln & 3) * 8; \
        v4i16_t lo = __builtin_amdgcn_ds_read_tr16_b64_v4i16((LAS v4i16_t*)(rb)); \
        v4i16_t hi = __builtin_amdgcn_ds_read_tr16_b64_v4i16((LAS v4i16_t*)(rb + (nat ? 256 : 512))); \
        v8i16_t w = __builtin_shufflevector(lo, hi, 0, 1, 2, 3, 4, 5, 6, 7); \
          \
        if (rf < NFRAG) asm volatile("global_store_dwordx4 %0, %1, %2 sc1\n\ts_nop 1" :: "v"(ln * 16), "v"(w), "s"(ws + WS_W2 + (size_t)rf * 1024) : "memory"); \
    } while (0)
    if (wid < 8) {
        K1_ADDR();
        f32x16 acc = (f32x16){};
        K1Frag f0, f1;
        BAR();
        STAMP(ws, 0, 3);
        k1_ldfrag(f0, lds, 0, aoff, boff);
#define K1_STAGE(FC, FN, it) do { \
            if ((it) < 7) { asm volatile("s_waitcnt lgkmcnt(0)" ::: "memory"); BAR(); k1_ldfrag(FN, lds, (it) + 1, aoff, boff); } \
            A_MFMA_ACC(acc, FC.b[0], FC.a[0]); A_MFMA_ACC(acc, FC.b[1], FC.a[1]); \
            A_MFMA_ACC(acc, FC.b[2], FC.a[2]); A_MFMA_ACC(acc, FC.b[3], FC.a[3]); \
        } while (0)
        K1_STAGE(f0, f1, 0); K1_STAGE(f1, f0, 1);
        K1_RLOAD();
        K1_STAGE(f0, f1, 2); K1_STAGE(f1, f0, 3); K1_STAGE(f0, f1, 4);
        K1_REPACK();
        K1_STAGE(f1, f0, 5); K1_STAGE(f0, f1, 6); K1_STAGE(f1, f0, 7);
#undef K1_STAGE
        asm volatile("s_nop 7\n\ts_nop 7\n\ts_nop 3" : "+v"(acc));
        STAMP(ws, 0, 1);
        const int ix = 2 * xj + (ni & 1);
        if (ni < 2) acc = acc * (0.25f * 1.4426950408889634f);
        char* dst = ws + (ni < 2 ? WS_Q : WS_K);
#pragma unroll
        for (int s = 0; s < 2; ++s)
            *(u32x4*)(dst + ((size_t)(2 * ix + s) * 64 + tt) * 1024 + lane * 16) = pack8(acc, s);
    } else {
        const bool storex = ((mt >> 2) & 3) * 2 == xj;
        char* xf = ws + WS_XF + ((size_t)tt * NKS_D + (ni & 1) * 2) * 1024 + lane * 16;
        K1_ADDR();
        f32x16 acc = (f32x16){};
        K1Frag f0, f1;
        BAR();
        k1_ldfrag(f0, lds, 0, aoff, boff);
#define K1_STAGE(FC, FN, it) do { \
            if ((it) < 7) { asm volatile("s_waitcnt lgkmcnt(0)" ::: "memory"); BAR(); k1_ldfrag(FN, lds, (it) + 1, aoff, boff); } \
            A_MFMA_ACC(acc, FC.a[0], FC.b[0]); A_MFMA_ACC(acc, FC.a[1], FC.b[1]); \
            A_MFMA_ACC(acc, FC.a[2], FC.b[2]); A_MFMA_ACC(acc, FC.a[3], FC.b[3]); \
            if (storex) { char* xd_ = xf + (size_t)(((it) + s0) & 7) * 4096; \
                          if (ni & 1) { *(bf16x8*)(xd_) = FC.a[2]; *(bf16x8*)(xd_ + 1024) = FC.a[3]; } \
                          else { *(bf16x8*)(xd_) = FC.a[0]; *(bf16x8*)(xd_ + 1024) = FC.a[1]; } \
                          asm volatile("" ::: "memory"); } \
        } while (0)
        K1_STAGE(f0, f1, 0); K1_STAGE(f1, f0, 1);
        K1_RLOAD();
        K1_STAGE(f0, f1, 2); K1_STAGE(f1, f0, 3); K1_STAGE(f0, f1, 4);
        K1_REPACK();
        K1_STAGE(f1, f0, 5); K1_STAGE(f0, f1, 6); K1_STAGE(f1, f0, 7);
#undef K1_STAGE
        asm volatile("s_nop 7\n\ts_nop 7\n\ts_nop 3" : "+v"(acc));
        STAMP(ws, 0, 1);
        const int head = 2 * (2 * xj + (ni & 1)) + (r >> 4), d = r & 15;
#pragma unroll
        for (int s = 0; s < 2; ++s)
            *(u32x4*)(ws + WS_V + ((size_t)head * 128 + 2 * tt + s) * 512 + h * 256 + d * 16) = pack8(acc, s);
    }
#undef K1_ADDR
#undef K1_RLOAD
#undef K1_REPACK
    STAMP(ws, 0, 2);
}

#define X_NOLDS 0
#define X_NOMLP 0
constexpr int K2_CONST = 128 * 1024;
constexpr int K2_RED = K2_CONST + 20 * 512;
constexpr int K2_FLAG = K2_RED + 16384;
constexpr int K2_LDS = K2_FLAG + 64;
constexpr float K2_LIM = 65536.f;
constexpr int K2_PF = 8;

#define A_MFMA_S(D, K) asm volatile("v_mfma_f32_32x32x16_bf16 %0, %1, %2, %3" : "=&v"(D) : "v"(K), "v"(qf), "v"(negm) : "memory")
#define A_MFMA_PV(V, P) asm volatile("v_mfma_f32_32x32x16_bf16 %0, %1, %2, %0" : "+v"(og) : "v"(V), "v"(P))
#define A_MFMA_PV0(V, P) asm volatile("v_mfma_f32_32x32x16_bf16 %0, %1, %2, 0" : "=&v"(og) : "v"(V), "v"(P))
#define A_EXP4(E, S, i) asm volatile("v_exp_f32 %0, %4\n\tv_exp_f32 %1, %5\n\tv_exp_f32 %2, %6\n\tv_exp_f32 %3, %7" \
        : "=&v"(E[i]), "=&v"(E[i + 1]), "=&v"(E[i + 2]), "=&v"(E[i + 3]) : "v"(S[i]), "v"(S[i + 1]), "v"(S[i + 2]), "v"(S[i + 3]))
#define A_CVT4(P, E, i) asm volatile("v_cvt_pk_bf16_f32 %0, %4, %5\n\tv_cvt_pk_bf16_f32 %1, %6, %7\n\tv_cvt_pk_bf16_f32 %2, %8, %9\n\tv_cvt_pk_bf16_f32 %3, %10, %11" \
        : "=&v"(P[0]), "=&v"(P[1]), "=&v"(P[2]), "=&v"(P[3]) \
        : "v"(E[i]), "v"(E[i + 1]), "v"(E[i + 2]), "v"(E[i + 3]), "v"(E[i + 4]), "v"(E[i + 5]), "v"(E[i + 6]), "v"(E[i + 7]))

__global__ __launch_bounds__(768) void k2_kernel(char* __restrict__ ws, const u16* __restrict__ b1, const u16* __restrict__ wo,
                                                 const u16* __restrict__ x, const u16* __restrict__ gw) {
    extern __shared__ __attribute__((aligned(16))) char smem[];
    LAS char* lds = (LAS char*)smem;
    const int tid = threadIdx.x, lane = tid & 63;
    const int wid = __builtin_amdgcn_readfirstlane(tid >> 6);
    const int bid = blockIdx.x;
    const int r = lane & 31, h = lane >> 5;
    STAMPL(0);

    const int head = ((bid >> 1) & 3) * 8 + ((bid >> 3) & 7), qb = (bid & 1) * 4 + (bid >> 6);
    const int qt = qb * 8 + (wid & 7);
    const int c0 = (bid & 1) * 4;
    bf16x8 qf;

    const char* kg = ws + WS_K + (size_t)head * 65536 + (wid & 7) * 1024;
    const char* vg = ws + WS_V + (size_t)head * 65536 + (wid & 7) * 1024;
    const unsigned ldsw = (unsigned)(uintptr_t)lds + (wid & 7) * 1024;
#define K2_CHUNK(c) do { const int cs_ = (((c) + c0) & 7) * 8192; \
        glds16(kg + cs_, lane * 16, ldsw + (c) * 8192); glds16(vg + cs_, lane * 16, ldsw + 65536 + (c) * 8192); } while (0)
    if (wid < 8) {
        asm volatile("global_load_dwordx4 %0, %1, %2" : "=v"(qf) : "v"(lane * 16), "s"(ws + WS_Q + ((size_t)head * 64 + qt) * 1024) : "memory");
        K2_CHUNK(0); K2_CHUNK(1); K2_CHUNK(2); K2_CHUNK(3);
    }
    if (tid < 4) { *(LAS u32x4*)(lds + K2_FLAG + tid * 16) = (u32x4){0u, 0u, 0u, 0u}; }
    asm volatile("s_waitcnt lgkmcnt(0)" ::: "memory");
    BAR();

    if (wid >= 8) {
        const int tb = (bid & 1) * 16 + ((bid >> 1) & 3) * 4 + ((bid >> 3) & 3), nb = bid >> 5;
        const int ni = wid & 1, kh = (wid >> 1) & 1;
        __builtin_amdgcn_s_setprio(3);
        const unsigned vo = lane * 16;
        volatile LAS unsigned* lw = (volatile LAS unsigned*)(lds + K2_FLAG);
        while (lw[9] < 8u) __builtin_amdgcn_s_sleep(4);
#define M_LOAD(D, sb) asm volatile("global_load_dwordx4 %0, %1, %2" : "=v"(D) : "v"(vo), "s"(sb) : "memory")
#define M_LOAD_SC1(D, sb) asm volatile("global_load_dwordx4 %0, %1, %2 sc1" : "=v"(D) : "v"(vo), "s"(sb) : "memory")
#define M_GAP() asm volatile("s_nop 7\n\ts_nop 3")
#define M_MFMA0(C, A, B) asm volatile("v_mfma_f32_32x32x16_bf16 %0, %1, %2, 0" : "=&v"(C) : "v"(A), "v"(B))
        const char* wf = ws + WS_W2 + ((size_t)(nb * 2 + ni) * NKS_F + kh * 43) * 1024;
        const char* af = ws + WS_ACT + ((size_t)(tb * 2) * NKS_F + kh * 43) * 1024;
        const int k2o = (nb * 5 + (tb & 3) * 11) % 43;
        bf16x8 fw[K2_PF], fa0[K2_PF], fa1[K2_PF];
        {
            const int wv = nb * 4 + (wid & 3), j = wv & 1, slot = wv >> 1;
            const int ft0 = slot < 11 ? 3 * slot : 33 + 2 * (slot - 11);
            const int ft2 = slot < 11 ? ft0 + 2 : ft0 + 1;
            const char* xb = ws + WS_XF + (size_t)(tb * 2 + j) * (NKS_D * 1024);
            const char* wb0 = ws + WS_W1 + (size_t)ft0 * (NKS_D * 1024);
            const char* wb2 = ws + WS_W1 + (size_t)ft2 * (NKS_D * 1024);
            const int k1o = ((tb & 3) * 2 + j) * 4;
            bf16x8 fx[6], f0[6], f1[6], f2[6];
            int kq1 = k1o;
#define P1_ISSUE(i) do { const int kk_ = kq1 * 1024; kq1 = (kq1 + 1) & 31; \
                M_LOAD(fx[i], xb + kk_); M_LOAD(f0[i], wb0 + kk_); M_LOAD(f1[i], wb0 + NKS_D * 1024 + kk_); M_LOAD(f2[i], wb2 + kk_); } while (0)
#define P1_STEP(i, n) do { asm volatile("s_waitcnt vmcnt(" #n ")" : "+v"(fx[i]), "+v"(f0[i]), "+v"(f1[i]), "+v"(f2[i]) :: "memory"); \
                A_MFMA_ACC(c[0], f0[i], fx[i]); M_GAP(); A_MFMA_ACC(c[1], f1[i], fx[i]); M_GAP(); A_MFMA_ACC(c[2], f2[i], fx[i]); } while (0)
            P1_ISSUE(0); P1_ISSUE(1); P1_ISSUE(2); P1_ISSUE(3); P1_ISSUE(4); P1_ISSUE(5);
            f32x16 c[3];
#pragma unroll
            for (int t = 0; t < 3; ++t) {
                const unsigned* bs = (const unsigned*)(b1 + 32 * (t < 2 ? ft0 + t : ft2));
#pragma unroll
                for (int e = 0; e < 8; ++e) {
                    const int d0 = (e & 1) + 4 * (e >> 1);
                    const unsigned bw = h ? bs[d0 + 2] : bs[d0];
                    c[t][2 * e] = __uint_as_float(bw << 16); c[t][2 * e + 1] = __uint_as_float(bw & 0xffff0000u);
                }
            }
            asm volatile("s_nop 1" : "+v"(c[0]), "+v"(c[1]), "+v"(c[2]));
            for (int rd = 0; rd < 4; ++rd) {
                P1_STEP(0, 20); P1_ISSUE(0); P1_STEP(1, 20); P1_ISSUE(1); P1_STEP(2, 20); P1_ISSUE(2);
                P1_STEP(3, 20); P1_ISSUE(3); P1_STEP(4, 20); P1_ISSUE(4); P1_STEP(5, 20); P1_ISSUE(5);
            }
            P1_STEP(0, 20); P1_ISSUE(0); P1_STEP(1, 20); P1_ISSUE(1);
            P1_STEP(2, 20); P1_STEP(3, 16); P1_STEP(4, 12); P1_STEP(5, 8); P1_STEP(0, 4); P1_STEP(1, 0);
#undef P1_ISSUE
#undef P1_STEP
            {
                int kk = k2o;
#pragma unroll
                for (int k = 0; k < K2_PF; ++k) { M_LOAD(fw[k], wf + kk * 1024); kk = kk == 42 ? 0 : kk + 1; }
            }
            asm volatile("s_nop 7\n\ts_nop 7\n\ts_nop 3" : "+v"(c[0]), "+v"(c[1]), "+v"(c[2]));
            STAMPL(4);
#pragma unroll
            for (int t = 0; t < 3; ++t) {
                const int ft = t < 2 ? ft0 + t : ft2;
                f32x16 a = c[t];
#pragma unroll
                for (int e = 0; e < 16; ++e) a[e] = mish_f(a[e]);
                const char* dst = ws + WS_ACT + ((size_t)(tb * 2 + j) * NKS_F + 2 * ft) * 1024;
                const u32x4 p0 = pack8(a, 0), p1 = pack8(a, 1);
                asm volatile("global_store_dwordx4 %0, %1, %2 sc1\n\ts_nop 1" :: "v"(vo), "v"(p0), "s"(dst) : "memory");
                asm volatile("global_store_dwordx4 %0, %1, %2 offset:1024 sc1\n\ts_nop 1" :: "v"(vo), "v"(p1), "s"(dst) : "memory");
            }
                const int f4 = ((bid >> 3) * 4 + (wid & 3)) * 4;
                const unsigned scr = (unsigned)(uintptr_t)lds + K2_RED + (wid & 3) * 4096;
                const char* wsrc = (const char*)(wo + (size_t)(16 * (f4 & 31)) * D_ + (f4 >> 5) * 32);
#pragma unroll
                for (int i = 0; i < 4; ++i) glds16(wsrc + i * (16 * D_ * 2), ((lane >> 2) * D_ + (lane & 3) * 8) * 2, scr + i * 1024);
                const int tok = (bid * 4 + (wid & 3)) * 2;
                u32x4 gx0, gx1, gg;
                M_LOAD(gx0, x + (size_t)tok * D_); M_LOAD(gx1, x + (size_t)(tok + 1) * D_); M_LOAD(gg, gw);
            asm volatile("s_waitcnt vmcnt(0)" : "+v"(fw[0]), "+v"(fw[1]), "+v"(fw[2]), "+v"(fw[3]), "+v"(fw[4]), "+v"(fw[5]), "+v"(fw[6]), "+v"(fw[7]),
                         "+v"(gx0), "+v"(gx1), "+v"(gg) :: "memory");
            STAMPL(5);
            unsigned old = 0;
            if (lane == 0) old = __hip_atomic_fetch_add((LAS unsigned*)(lw + 4), 1u, __ATOMIC_RELAXED, __HIP_MEMORY_SCOPE_WORKGROUP);
            const bool last = __builtin_amdgcn_readfirstlane(old) == 3u;
            GAS unsigned* cnt = (GAS unsigned*)(ws + WS_CNT) + tb * 64;
            if (last && lane == 0) __hip_atomic_fetch_add(cnt, 1u, __ATOMIC_RELAXED, __HIP_MEMORY_SCOPE_AGENT);
            {
                const LAS char* rb = lds + K2_RED + (wid & 3) * 4096 + (4 * h + ((lane & 15) >> 2)) * 64 + ((lane >> 4) & 1) * 32 + (lane & 3) * 8;
#pragma unroll
                for (int i = 0; i < 4; ++i) {
                    v4i16_t lo = __builtin_amdgcn_ds_read_tr16_b64_v4i16((LAS v4i16_t*)(rb + i * 1024));
                    v4i16_t hi = __builtin_amdgcn_ds_read_tr16_b64_v4i16((LAS v4i16_t*)(rb + i * 1024 + 512));
                    *(v8i16_t*)(ws + WS_WOX + (size_t)(bid & 7) * (16 * NKS_D * 1024) + (size_t)(f4 + i) * 1024 + vo) =
                        __builtin_shufflevector(lo, hi, 0, 1, 2, 3, 4, 5, 6, 7);
                }
                float s0_ = 0.f, s1_ = 0.f;
#pragma unroll
                for (int e = 0; e < 4; ++e) {
                    const float gl = __uint_as_float(gg[e] << 16), gh = __uint_as_float(gg[e] & 0xffff0000u);
                    s0_ += __uint_as_float(gx0[e] << 16) * gl + __uint_as_float(gx0[e] & 0xffff0000u) * gh;
                    s1_ += __uint_as_float(gx1[e] << 16) * gl + __uint_as_float(gx1[e] & 0xffff0000u) * gh;
                }
                float sv = h ? s1_ : s0_;
                sv += __shfl_xor(h ? s0_ : s1_, 32);
#pragma unroll
                for (int o = 16; o > 0; o >>= 1) sv += __shfl_xor(sv, o);
                if (r == 0) ((float*)(ws + WS_G))[tok + h] = 1.f / (1.f + __expf(-sv));
            }
            if (last) {
                for (unsigned spins = 0; spins < (1u << 22); ++spins) {
                    if ((unsigned)__builtin_amdgcn_readfirstlane(__hip_atomic_load(cnt, __ATOMIC_RELAXED, __HIP_MEMORY_SCOPE_AGENT)) >= 8u) break;
                    __builtin_amdgcn_s_sleep(4);
                }
                asm volatile("s_waitcnt vmcnt(0)" ::: "memory");
                if (lane == 0) lw[5] = 1u;
            } else {
                while (lw[5] == 0u) __builtin_amdgcn_s_sleep(2);
            }
            asm volatile("s_waitcnt vmcnt(0) lgkmcnt(0)" ::: "memory");
            if (lane == 0) __hip_atomic_fetch_add((LAS unsigned*)(lw + 6), 1u, __ATOMIC_RELAXED, __HIP_MEMORY_SCOPE_WORKGROUP);
            STAMPL(6);
        }
        STAMPL(1);
        f32x16 c0, c1;
        int kq2 = k2o;
#pragma unroll
        for (int k = 0; k < K2_PF; ++k) { M_LOAD_SC1(fa0[k], af + kq2 * 1024); M_LOAD_SC1(fa1[k], af + (NKS_F + kq2) * 1024); kq2 = kq2 == 42 ? 0 : kq2 + 1; }
#define P2_ISSUE(i) do { const int kk_ = kq2 * 1024; kq2 = kq2 == 42 ? 0 : kq2 + 1; \
            M_LOAD(fw[i], wf + kk_); M_LOAD_SC1(fa0[i], af + kk_); M_LOAD_SC1(fa1[i], af + NKS_F * 1024 + kk_); } while (0)
#define P2_WAIT(i, n) asm volatile("s_waitcnt vmcnt(" #n ")" : "+v"(fw[i]), "+v"(fa0[i]), "+v"(fa1[i]) :: "memory")
#define P2_STEP(i, n) do { P2_WAIT(i, n); A_MFMA_ACC(c0, fw[i], fa0[i]); M_GAP(); A_MFMA_ACC(c1, fw[i], fa1[i]); } while (0)
        P2_WAIT(0, 14); M_MFMA0(c0, fw[0], fa0[0]); M_MFMA0(c1, fw[0], fa1[0]); P2_ISSUE(0);
        P2_STEP(1, 15); P2_ISSUE(1); P2_STEP(2, 16); P2_ISSUE(2); P2_STEP(3, 17); P2_ISSUE(3);
        P2_STEP(4, 18); P2_ISSUE(4); P2_STEP(5, 19); P2_ISSUE(5); P2_STEP(6, 20); P2_ISSUE(6); P2_STEP(7, 21); P2_ISSUE(7);
        for (int rd = 0; rd < 3; ++rd) {
            P2_STEP(0, 21); P2_ISSUE(0); P2_STEP(1, 21); P2_ISSUE(1); P2_STEP(2, 21); P2_ISSUE(2); P2_STEP(3, 21); P2_ISSUE(3);
            P2_STEP(4, 21); P2_ISSUE(4); P2_STEP(5, 21); P2_ISSUE(5); P2_STEP(6, 21); P2_ISSUE(6); P2_STEP(7, 21); P2_ISSUE(7);
        }
        P2_STEP(0, 21); P2_ISSUE(0); P2_STEP(1, 21); P2_ISSUE(1); P2_STEP(2, 21); P2_ISSUE(2);
        P2_STEP(3, 21); P2_STEP(4, 18); P2_STEP(5, 15); P2_STEP(6, 12); P2_STEP(7, 9); P2_STEP(0, 6); P2_STEP(1, 3); P2_STEP(2, 0);
#undef P2_ISSUE
#undef P2_WAIT
#undef P2_STEP
        asm volatile("s_nop 7\n\ts_nop 7\n\ts_nop 3" : "+v"(c0), "+v"(c1));
        LAS f32x4* red = (LAS f32x4*)(lds + K2_RED + ni * 8192);
        volatile LAS unsigned* flag = (volatile LAS unsigned*)(lds + K2_FLAG) + ni;
        if (kh == 1) {
            while (lw[6] < 4u) __builtin_amdgcn_s_sleep(1);
            asm volatile("" ::: "memory");
#pragma unroll
            for (int qd = 0; qd < 4; ++qd) {
                red[qd * 64 + lane] = (f32x4){c0[4 * qd], c0[4 * qd + 1], c0[4 * qd + 2], c0[4 * qd + 3]};
                red[(4 + qd) * 64 + lane] = (f32x4){c1[4 * qd], c1[4 * qd + 1], c1[4 * qd + 2], c1[4 * qd + 3]};
            }
            asm volatile("s_waitcnt lgkmcnt(0)" ::: "memory");
            if (lane == 0) *flag = 1u;
        } else {
            while (*flag == 0u) __builtin_amdgcn_s_sleep(2);
            asm volatile("" ::: "memory");
            char* dst = ws + WS_B + ((size_t)(tb * 8 + nb) * 4 + ni * 2) * 2048 + lane * 16;
#pragma unroll
            for (int qd = 0; qd < 4; ++qd) {
                const f32x4 u0 = red[qd * 64 + lane], u1 = red[(4 + qd) * 64 + lane];
#pragma unroll
                for (int e = 0; e < 4; ++e) { c0[4 * qd + e] += u0[e]; c1[4 * qd + e] += u1[e]; }
            }
#pragma unroll
            for (int s = 0; s < 2; ++s) {
                *(u32x4*)(dst + s * 1024) = pack8(c0, s);
                *(u32x4*)(dst + 2048 + s * 1024) = pack8(c1, s);
            }
        }
        STAMPL(3);
        STAMPL_FLUSH(ws, 1);
        return;
    }

    if (tid < 40) {
        const unsigned c = (tid & 1) ? 0u : 0x3f803f80u;
        *(LAS u32x4*)(lds + K2_CONST + (tid >> 1) * 512 + (tid & 1) * 16) = (u32x4){c, c, c, c};
    }
    const bool vact = r < 16;
    const LAS char* kp = lds + lane * 16;
    const LAS char* vp = vact ? lds + 65536 + h * 256 + r * 16 : lds + K2_CONST + (r == 16 ? 0 : 16);
    const int vinc = vact ? 8192 : 0;

    f32x16 ot = (f32x16){};
    f32x16 negm, sa, sb;
    bf16x8 ka, kb, va0, va1, vb0, vb1;
    if (X_NOLDS) { vb0 = *(const LAS bf16x8*)(vp); vb1 = *(const LAS bf16x8*)(vp + 512); }
    float m;
    volatile LAS unsigned* cw = (volatile LAS unsigned*)(lds + K2_FLAG) + 8;
#define K2_SIGNAL(c) do { if (lane == 0) __hip_atomic_fetch_add((LAS unsigned*)(cw + (c)), 1u, __ATOMIC_RELAXED, __HIP_MEMORY_SCOPE_WORKGROUP); } while (0)
#define K2_NEED(c) do { while (cw[c] < 8u) __builtin_amdgcn_s_sleep(1); asm volatile("" ::: "memory"); } while (0)
    asm volatile("s_waitcnt vmcnt(2) lgkmcnt(0)" : "+v"(qf) :: "memory");
    K2_CHUNK(4); K2_CHUNK(5); K2_CHUNK(6); K2_CHUNK(7);
    K2_SIGNAL(1);
    K2_NEED(1);
    STAMPL(1);
    {
        ka = *(const LAS bf16x8*)kp;
        kb = *(const LAS bf16x8*)(kp + 1024);
        va0 = *(const LAS bf16x8*)(vp); va1 = *(const LAS bf16x8*)(vp + 512);
        f32x16 s = MFMA(ka, qf, (f32x16){});
        float mx = fmaxf(s[0], s[1]);
#pragma unroll
        for (int e = 2; e < 16; ++e) mx = fmaxf(mx, s[e]);
        mx = fmaxf(mx, __shfl_xor(mx, 32));
        m = mx;
#pragma unroll
        for (int e = 0; e < 16; ++e) negm[e] = -m;
        asm volatile("s_nop 3" : "+v"(negm));
        A_MFMA_S(sa, ka);
        asm volatile("s_nop 7\n\ts_nop 7" : "+v"(sa));
    }
    const LAS char* kq = kp;
    const LAS char* vq = vp;
    int g = 0;
    for (; g < 8; ++g) {
        if (g == 1) { WAIT_V(0); K2_SIGNAL(2); }
        else if (g == 2) K2_NEED(2);
        if (g == 0) __builtin_amdgcn_s_setprio(2);
        else if (g == 5) __builtin_amdgcn_s_setprio(1);
        else if (g == 7) __builtin_amdgcn_s_setprio(0);
        const LAS char* kq2 = (g == 7) ? kp - 8192 : kq;
        const LAS char* vq2 = (g == 7) ? vp - vinc : vq;
        asm volatile("" : "+v"(kq), "+v"(vq), "+v"(kq2), "+v"(vq2));
        f32x16 og;
        float e[16];
        u32x4 p0, p1;
#define K2_TILE(SC, SN, KC, KN, VC0, VP1, VN0, VN1, t) do { \
            if ((t) > 0) A_MFMA_PV(VP1, p1); \
            if (!X_NOLDS) { \
            KN = ((t) + 2 < 8) ? *(const LAS bf16x8*)(kq + ((t) + 2) * 1024) : *(const LAS bf16x8*)(kq2 + ((t) + 2) * 1024); \
            if ((t) + 1 < 8) { VN0 = *(const LAS bf16x8*)(vq + ((t) + 1) * 1024); VN1 = *(const LAS bf16x8*)(vq + ((t) + 1) * 1024 + 512); } \
            else { VN0 = *(const LAS bf16x8*)(vq2 + ((t) + 1) * 1024); VN1 = *(const LAS bf16x8*)(vq2 + ((t) + 1) * 1024 + 512); } } \
            A_EXP4(e, SC, 0); A_EXP4(e, SC, 4); \
            A_MFMA_S(SN, KC); \
            A_CVT4(p0, e, 0); \
            A_EXP4(e, SC, 8); \
            if ((t) == 0) A_MFMA_PV0(VC0, p0); else A_MFMA_PV(VC0, p0); \
            A_EXP4(e, SC, 12); \
            A_CVT4(p1, e, 8); \
            asm volatile("s_nop 1"); \
        } while (0)
#pragma unroll
        for (int t = 0; t < 8; t += 2) {
            K2_TILE(sa, sb, kb, ka, va0, vb1, vb0, vb1, t);
            K2_TILE(sb, sa, ka, kb, vb0, va1, va0, va1, t + 1);
        }
        A_MFMA_PV(vb1, p1);
        asm volatile("s_nop 7\n\ts_nop 7\n\ts_nop 3" : "+v"(og));
#undef K2_TILE
        if (__builtin_expect(__any(!(og[8] <= K2_LIM)), 0)) break;
#pragma unroll
        for (int i = 0; i < 9; ++i) ot[i] += og[i];
        kq += 8192; vq += vinc;
    }
    if (__builtin_expect(g < 8, 0)) {
        WAIT_V(0);
        if (g < 1) K2_SIGNAL(2);
        K2_NEED(2);
        for (int t = g * 8; t < 64; ++t) {
            bf16x8 kf = *(const LAS bf16x8*)(kp + t * 1024);
            const LAS char* vt = vp + (vact ? t * 1024 : 0);
            vb0 = *(const LAS bf16x8*)(vt); vb1 = *(const LAS bf16x8*)(vt + 512);
            f32x16 s = MFMA(kf, qf, (f32x16){});
            float mx = fmaxf(s[0], s[1]);
#pragma unroll
            for (int i = 2; i < 16; ++i) mx = fmaxf(mx, s[i]);
            mx = fmaxf(mx, __shfl_xor(mx, 32));
            float mn = fmaxf(m, mx);
            float al = __builtin_amdgcn_exp2f(m - mn);
            m = mn;
            ot = ot * al;
#pragma unroll
            for (int i = 0; i < 16; ++i) s[i] = __builtin_amdgcn_exp2f(s[i] - m);
            ot = MFMA(vb0, __builtin_bit_cast(bf16x8, pack8(s, 0)), ot);
            ot = MFMA(vb1, __builtin_bit_cast(bf16x8, pack8(s, 1)), ot);
        }
    }
    STAMPL(2);
    float l = __shfl(ot[8], r);
    float rl = 1.f / l;
    f32x16 on = ot * rl;
    *(u32x4*)(ws + WS_O + ((size_t)qt * NKS_D + head) * 1024 + lane * 16) = pack8(on, 0);
    STAMPL(3);
    STAMPL_FLUSH(ws, 1);
}

constexpr int K3_LDS = 64 * 1024;

#define A_GLOAD(D, voff, sbase, imm) asm volatile("global_load_dwordx4 %0, %1, %2 offset:" #imm : "=v"(D) : "v"(voff), "s"(sbase) : "memory")

__global__ __launch_bounds__(256) void k3_kernel(const char* __restrict__ ws, u16* __restrict__ out) {
    extern __shared__ __attribute__((aligned(16))) char smem[];
    const int tid = threadIdx.x, lane = tid & 63;
    const int wid = __builtin_amdgcn_readfirstlane(tid >> 6);
    const int bid = blockIdx.x;
    const int tt = (bid & 1) * 32 + ((bid >> 1) & 3) * 8 + ((bid >> 3) & 7), nq = bid >> 6;
    const int r = lane & 31, h = lane >> 5;
    STAMP(ws, 2, 0);

    const char* af = ws + WS_O + ((size_t)tt * NKS_D + wid * 8) * 1024;
    const char* wf = ws + WS_WOX + (size_t)(bid & 7) * (16 * NKS_D * 1024) + ((size_t)(nq * 4) * NKS_D + wid * 8) * 1024;
    const unsigned vo = lane * 16, vo4 = lane * 16 + 4096;
    bf16x8 a[8], w[4][8];
    A_GLOAD(a[0], vo, af, 0); A_GLOAD(a[1], vo, af, 1024); A_GLOAD(a[2], vo, af, 2048); A_GLOAD(a[3], vo, af, 3072);
    A_GLOAD(a[4], vo4, af, 0); A_GLOAD(a[5], vo4, af, 1024); A_GLOAD(a[6], vo4, af, 2048); A_GLOAD(a[7], vo4, af, 3072);
#pragma unroll
    for (int t = 0; t < 4; ++t) {
        const char* wt = wf + (size_t)t * NKS_D * 1024;
        A_GLOAD(w[t][0], vo, wt, 0); A_GLOAD(w[t][1], vo, wt, 1024); A_GLOAD(w[t][2], vo, wt, 2048); A_GLOAD(w[t][3], vo, wt, 3072);
        A_GLOAD(w[t][4], vo4, wt, 0); A_GLOAD(w[t][5], vo4, wt, 1024); A_GLOAD(w[t][6], vo4, wt, 2048); A_GLOAD(w[t][7], vo4, wt, 3072);
    }
    const int nt = nq * 4 + wid;
    const char* bp = ws + WS_B + ((size_t)((tt >> 1) * 8 + (nt >> 1)) * 4 + (nt & 1) * 2 + (tt & 1)) * 2048 + lane * 16;
    const u32x4 bq0 = *(const u32x4*)(bp), bq1 = *(const u32x4*)(bp + 1024);
    const float g = ((const float*)(ws + WS_G))[tt * 32 + r];

    f32x16 acc[4];
#define K3_MM(t) do { asm volatile("v_mfma_f32_32x32x16_bf16 %0, %1, %2, 0" : "=&v"(acc[t]) : "v"(w[t][0]), "v"(a[0])); \
        _Pragma("unroll") for (int i = 1; i < 8; ++i) A_MFMA_ACC(acc[t], w[t][i], a[i]); } while (0)
#define K3_W(t, n) asm volatile("s_waitcnt vmcnt(" #n ")" : "+v"(w[t][0]), "+v"(w[t][1]), "+v"(w[t][2]), "+v"(w[t][3]), "+v"(w[t][4]), "+v"(w[t][5]), "+v"(w[t][6]), "+v"(w[t][7]) :: "memory")
    asm volatile("s_waitcnt vmcnt(35)" : "+v"(a[0]), "+v"(a[1]), "+v"(a[2]), "+v"(a[3]), "+v"(a[4]), "+v"(a[5]), "+v"(a[6]), "+v"(a[7]) :: "memory");
    K3_W(0, 27);
    K3_MM(0);
    K3_W(1, 19);
    K3_MM(1);
    K3_W(2, 11);
    K3_MM(2);
    K3_W(3, 3);
    K3_MM(3);
#undef K3_W
#undef K3_MM
    asm volatile("s_nop 7\n\ts_nop 7\n\ts_nop 3" : "+v"(acc[0]), "+v"(acc[1]), "+v"(acc[2]), "+v"(acc[3]));
    STAMP(ws, 2, 1);
    f32x4* red = (f32x4*)smem;
#pragma unroll
    for (int t = 0; t < 4; ++t)
#pragma unroll
        for (int qd = 0; qd < 4; ++qd)
            red[((wid * 4 + t) * 4 + qd) * 64 + lane] = (f32x4){acc[t][4 * qd], acc[t][4 * qd + 1], acc[t][4 * qd + 2], acc[t][4 * qd + 3]};
    __syncthreads();
    const int tok = tt * 32 + r;
#pragma unroll
    for (int qd = 0; qd < 4; ++qd) {
        f32x4 sacc = red[(wid * 4 + qd) * 64 + lane];
#pragma unroll
        for (int k = 1; k < 4; ++k) sacc += red[((k * 4 + wid) * 4 + qd) * 64 + lane];
        const unsigned b0 = (qd < 2 ? bq0 : bq1)[(qd & 1) * 2], b1_ = (qd < 2 ? bq0 : bq1)[(qd & 1) * 2 + 1];
        sacc = sacc * g + (f32x4){__uint_as_float(b0 << 16), __uint_as_float(b0 & 0xffff0000u), __uint_as_float(b1_ << 16), __uint_as_float(b1_ & 0xffff0000u)} * (1.f - g);
        const int n = nt * 32 + 8 * qd + 4 * h;
        u32x2 o = {pk2(sacc[0], sacc[1]), pk2(sacc[2], sacc[3])};
        *(u32x2*)(out + (size_t)tok * D_ + n) = o;
    }
    STAMP(ws, 2, 2);
}

extern "C" void kernel_launch(void* const* d_in, const int* in_sizes, int n_in,
                              void* d_out, int out_size, void* d_ws, size_t ws_size,
                              hipStream_t stream) {
    const u16* x = (const u16*)d_in[0];
    const u16* wqkv = (const u16*)d_in[1];
    const u16* wo = (const u16*)d_in[2];
    const u16* w1 = (const u16*)d_in[4];
    const u16* b1 = (const u16*)d_in[5];
    const u16* w2 = (const u16*)d_in[6];
    const u16* gw = (const u16*)d_in[7];
    char* ws = (char*)d_ws;
    (void)hipFuncSetAttribute((const void*)k1_kernel, hipFuncAttributeMaxDynamicSharedMemorySize, K1_LDS);
    (void)hipFuncSetAttribute((const void*)k2_kernel, hipFuncAttributeMaxDynamicSharedMemorySize, K2_LDS);
    (void)hipFuncSetAttribute((const void*)k3_kernel, hipFuncAttributeMaxDynamicSharedMemorySize, K3_LDS);
    k1_kernel<<<256, 1024, K1_LDS, stream>>>(x, wqkv, w1, w2, ws);
    k2_kernel<<<256, 768, K2_LDS, stream>>>(ws, b1, wo, x, gw);
    k3_kernel<<<256, 256, K3_LDS, stream>>>(ws, (u16*)d_out);
}
```

```cpp
#include <hip/hip_runtime.h>
#include <hip/hip_bf16.h>
#include <stdint.h>

typedef __attribute__((ext_vector_type(8))) __bf16 bf16x8;
typedef __attribute__((ext_vector_type(2))) __bf16 bf16x2;
typedef __attribute__((ext_vector_type(2))) float f32x2;
typedef __attribute__((ext_vector_type(4))) float f32x4;
typedef __attribute__((ext_vector_type(16))) float f32x16;
typedef __attribute__((ext_vector_type(4))) unsigned u32x4;
typedef __attribute__((ext_vector_type(2))) unsigned u32x2;
typedef short v4i16_t __attribute__((ext_vector_type(4)));
typedef short v8i16_t __attribute__((ext_vector_type(8)));
typedef unsigned short u16;
#define LAS __attribute__((address_space(3)))
#define GAS __attribute__((address_space(1)))
#define DI __device__ __forceinline__
#define MFMA(a, b, c) __builtin_amdgcn_mfma_f32_32x32x16_bf16((a), (b), (c), 0, 0, 0)

constexpr int S_ = 2048, D_ = 512, H_ = 32, F_ = 1376;
constexpr int NKS_F = F_ / 16;
constexpr int NKS_D = D_ / 16;

constexpr size_t WS_Q = 0;
constexpr size_t WS_K = WS_Q + (size_t)2 * 1024 * 1024;
constexpr size_t WS_V = WS_K + (size_t)2 * 1024 * 1024;
constexpr size_t WS_O = WS_V + (size_t)2 * 1024 * 1024;
constexpr size_t WS_ACT = WS_O + (size_t)2 * 1024 * 1024;
constexpr size_t WS_W2 = WS_ACT + (size_t)64 * NKS_F * 1024;
constexpr size_t WS_W1 = WS_W2 + (size_t)16 * NKS_F * 1024;
constexpr size_t WS_XF = WS_W1 + (size_t)43 * NKS_D * 1024;
constexpr size_t WS_CNT = WS_XF + (size_t)64 * NKS_D * 1024;
constexpr size_t WS_G = WS_CNT + 40 * 256;

DI unsigned pk2(float a, float b) { bf16x2 r = __builtin_convertvector((f32x2){a, b}, bf16x2); return __builtin_bit_cast(unsigned, r); }
DI float bf2f(u16 b) { return __uint_as_float((unsigned)b << 16); }
DI int crow(int r, int h) { return (r & 3) + 8 * (r >> 2) + 4 * h; }
DI u32x4 pack8(const f32x16& x, int s) {
    u32x4 p;
    p[0] = pk2(x[8 * s + 0], x[8 * s + 1]); p[1] = pk2(x[8 * s + 2], x[8 * s + 3]);
    p[2] = pk2(x[8 * s + 4], x[8 * s + 5]); p[3] = pk2(x[8 * s + 6], x[8 * s + 7]);
    return p;
}
DI void glds16(const char* sbase, unsigned voff, unsigned lds_base) {
    unsigned sv;
    asm volatile("s_mov_b32 %0, m0\n\ts_mov_b32 m0, %3\n\ts_nop 0\n\tglobal_load_lds_dwordx4 %1, %2\n\ts_mov_b32 m0, %0"
                 : "=&s"(sv) : "v"(voff), "s"(sbase), "s"(lds_base) : "memory");
}
#define STAMP(ws, kid, idx)
#define STAMPL(i)
#define STAMPL_FLUSH(ws, kid)
#define WAIT_V(n) asm volatile("s_waitcnt vmcnt(" #n ")" ::: "memory")
#define BAR() __builtin_amdgcn_s_barrier()

DI float mish_f(float h) {
    const float n = __builtin_amdgcn_exp2f(fminf(h, 20.f) * 1.4426950408889634f);
    const float w = n * (n + 2.f);
    return h * w * __builtin_amdgcn_rcpf(w + 2.f);
}

constexpr int K1_SA = 64 * 64 * 2;
constexpr int K1_SB = 3 * 64 * 64 * 2;
constexpr int K1_BOFF = 8 * K1_SA;
constexpr int K1_LDS = K1_BOFF + 4 * K1_SB;
constexpr int NFRAG = 16 * NKS_F + 43 * NKS_D;

#define A_MFMA_ACC(C, A, B) asm volatile("v_mfma_f32_32x32x16_bf16 %0, %1, %2, %0" : "+v"(C) : "v"(A), "v"(B))

struct K1Frag { bf16x8 a[4], b[4]; };
DI void k1_ldfrag(K1Frag& f, const LAS char* lds, int it, const int (&aoff)[4], int boff) {
    int ao_ = it * K1_SA, bo_ = (it & 3) * K1_SB;
    asm volatile("" : "+s"(ao_), "+s"(bo_));
#pragma unroll
    for (int ks = 0; ks < 4; ++ks) {
        f.a[ks] = *(const LAS bf16x8*)(lds + ao_ + aoff[ks]);
        v4i16_t lo = __builtin_amdgcn_ds_read_tr16_b64_v4i16((LAS v4i16_t*)(lds + bo_ + boff + ks * 2048));
        v4i16_t hi = __builtin_amdgcn_ds_read_tr16_b64_v4i16((LAS v4i16_t*)(lds + bo_ + boff + ks * 2048 + 512));
        v8i16_t w = __builtin_shufflevector(lo, hi, 0, 1, 2, 3, 4, 5, 6, 7);
        f.b[ks] = __builtin_bit_cast(bf16x8, w);
    }
}

__global__ __launch_bounds__(1024) void k1_kernel(const u16* __restrict__ x, const u16* __restrict__ wqkv, const u16* __restrict__ w1,
                                                  const u16* __restrict__ w2, char* __restrict__ ws) {
    extern __shared__ __attribute__((aligned(16))) char smem[];
    LAS char* lds = (LAS char*)smem;
    const int tid = threadIdx.x, lane = tid & 63;
    const int wid = __builtin_amdgcn_readfirstlane(tid >> 6);
    const int bid = blockIdx.x;
    const int xj = (bid & 6) + (bid >> 7), mt = (bid & 1) * 16 + ((bid >> 3) & 15);
    const int r = lane & 31, h = lane >> 5;
    const int s0 = mt & 7;
    const unsigned lds0 = (unsigned)(uintptr_t)lds;
    STAMP(ws, 0, 0);

    if (wid >= 12) {
        const int lw = wid - 12;
        const int row = lw * 16 + (lane >> 3);
        const char* abase = (const char*)(x + (size_t)(mt * 64) * D_);
        const char* bbase = (const char*)(wqkv + 64 * xj);
        const unsigned ao0 = (row * D_ + (((lane & 7) ^ ((row >> 1) & 7)) << 3)) * 2;
        const unsigned ao1 = ((row + 8) * D_ + (((lane & 7) ^ (((row + 8) >> 1) & 7)) << 3)) * 2;
        const unsigned bo0 = (row * (3 * D_) + (((lane & 7) ^ (((row >> 1) & 1) << 2)) << 3)) * 2;
        const unsigned bo1 = bo0 + 8 * (3 * D_) * 2;
        const unsigned adst = lds0 + lw * 2048, bdst = lds0 + K1_BOFF + lw * 2048;
#define K1_ISSUE_A(it) do { const int st_ = ((it) + s0) & 7; \
            glds16(abase + st_ * 128, ao0, adst + (it) * K1_SA); glds16(abase + st_ * 128, ao1, adst + (it) * K1_SA + 1024); } while (0)
#define K1_ISSUE_B(it) do { const int st_ = ((it) + s0) & 7; const int bf_ = ((it) & 3) * K1_SB; \
            const char* b_ = bbase + (size_t)st_ * (128 * 3 * D_); \
            _Pragma("unroll") for (int j_ = 0; j_ < 3; ++j_) { \
                glds16(b_ + j_ * (2 * D_), bo0, bdst + bf_ + j_ * 8192); glds16(b_ + j_ * (2 * D_), bo1, bdst + bf_ + j_ * 8192 + 1024); } } while (0)
        K1_ISSUE_A(0); K1_ISSUE_B(0); K1_ISSUE_A(1); K1_ISSUE_B(1);
        WAIT_V(8);  BAR(); K1_ISSUE_A(2); K1_ISSUE_B(2); K1_ISSUE_A(3); K1_ISSUE_B(3);
        WAIT_V(16); BAR(); K1_ISSUE_B(4); K1_ISSUE_A(4); K1_ISSUE_A(5);
        WAIT_V(18); BAR(); K1_ISSUE_B(5); K1_ISSUE_A(6); K1_ISSUE_A(7);
        WAIT_V(20); BAR(); K1_ISSUE_B(6);
        WAIT_V(18); BAR(); K1_ISSUE_B(7);
        WAIT_V(16); BAR();
        WAIT_V(6);  BAR();
        WAIT_V(0);  BAR();
#undef K1_ISSUE_A
#undef K1_ISSUE_B
        if (bid == 0 && lw == 0 && lane < 40) ((unsigned*)(ws + WS_CNT))[lane * 64] = 0u;
        return;
    }

    const int mi = wid & 1, ni = wid >> 1;
    const int tt = mt * 2 + mi;
    const int rf = bid * 12 + wid;
    const bool nat = rf >= 16 * NKS_F;
#define K1_ADDR() int ln = lane; asm volatile("" : "+v"(ln)); \
    const int arow = mi * 32 + (ln & 31), asw = (arow >> 1) & 7; \
    int aoff[4]; \
    _Pragma("unroll") for (int ks = 0; ks < 4; ++ks) aoff[ks] = arow * 128 + (((2 * ks + (ln >> 5)) ^ asw) << 4); \
    const int boff = K1_BOFF + (ni >> 1) * 8192 + (8 * (ln >> 5) + ((ln & 15) >> 2)) * 128 + (((ni & 1) ^ ((ln & 15) >> 3)) << 6) + ((ln >> 4) & 1) * 32 + (ln & 3) * 8;
#define K1_RLOAD() do { int f = rf < NFRAG ? rf : 0; const u16* wsrc; int ld; \
        if (f < 16 * NKS_F) { wsrc = w2 + (size_t)(16 * (f % NKS_F)) * D_ + (f / NKS_F) * 32; ld = D_; } \
        else { f -= 16 * NKS_F; wsrc = w1 + (size_t)(16 * (f & 31)) * F_ + (f >> 5) * 32; ld = F_; } \
        glds16((const char*)wsrc, ((ln >> 2) * ld + (ln & 3) * 8) * 2, lds0 + wid * 1024); } while (0)
#define K1_REPACK() do { asm volatile("s_waitcnt vmcnt(0)" ::: "memory"); \
        const LAS char* rb = lds + wid * 1024 + ((nat ? 8 : 4) * (ln >> 5) + ((ln & 15) >> 2)) * 64 + ((ln >> 4) & 1) * 32 + (ln & 3) * 8; \
        v4i16_t lo = __builtin_amdgcn_ds_read_tr16_b64_v4i16((LAS v4i16_t*)(rb)); \
        v4i16_t hi = __builtin_amdgcn_ds_read_tr16_b64_v4i16((LAS v4i16_t*)(rb + (nat ? 256 : 512))); \
        v8i16_t w = __builtin_shufflevector(lo, hi, 0, 1, 2, 3, 4, 5, 6, 7); \
          \
        if (rf < NFRAG) asm volatile("global_store_dwordx4 %0, %1, %2 sc1\n\ts_nop 1" :: "v"(ln * 16), "v"(w), "s"(ws + WS_W2 + (size_t)rf * 1024) : "memory"); \
    } while (0)
    if (wid < 8) {
        K1_ADDR();
        f32x16 acc = (f32x16){};
        K1Frag f0, f1;
        BAR();
        STAMP(ws, 0, 3);
        k1_ldfrag(f0, lds, 0, aoff, boff);
#define K1_STAGE(FC, FN, it) do { \
            if ((it) < 7) { asm volatile("s_waitcnt lgkmcnt(0)" ::: "memory"); BAR(); k1_ldfrag(FN, lds, (it) + 1, aoff, boff); } \
            A_MFMA_ACC(acc, FC.b[0], FC.a[0]); A_MFMA_ACC(acc, FC.b[1], FC.a[1]); \
            A_MFMA_ACC(acc, FC.b[2], FC.a[2]); A_MFMA_ACC(acc, FC.b[3], FC.a[3]); \
        } while (0)
        K1_STAGE(f0, f1, 0); K1_STAGE(f1, f0, 1);
        K1_RLOAD();
        K1_STAGE(f0, f1, 2); K1_STAGE(f1, f0, 3); K1_STAGE(f0, f1, 4);
        K1_REPACK();
        K1_STAGE(f1, f0, 5); K1_STAGE(f0, f1, 6); K1_STAGE(f1, f0, 7);
#undef K1_STAGE
        asm volatile("s_nop 7\n\ts_nop 7\n\ts_nop 3" : "+v"(acc));
        STAMP(ws, 0, 1);
        const int ix = 2 * xj + (ni & 1);
        if (ni < 2) acc = acc * (0.25f * 1.4426950408889634f);
        char* dst = ws + (ni < 2 ? WS_Q : WS_K);
#pragma unroll
        for (int s = 0; s < 2; ++s)
            *(u32x4*)(dst + ((size_t)(2 * ix + s) * 64 + tt) * 1024 + lane * 16) = pack8(acc, s);
    } else {
        const bool storex = ((mt >> 2) & 3) * 2 == xj;
        char* xf = ws + WS_XF + ((size_t)tt * NKS_D + (ni & 1) * 2) * 1024 + lane * 16;
        K1_ADDR();
        f32x16 acc = (f32x16){};
        K1Frag f0, f1;
        BAR();
        k1_ldfrag(f0, lds, 0, aoff, boff);
#define K1_STAGE(FC, FN, it) do { \
            if ((it) < 7) { asm volatile("s_waitcnt lgkmcnt(0)" ::: "memory"); BAR(); k1_ldfrag(FN, lds, (it) + 1, aoff, boff); } \
            A_MFMA_ACC(acc, FC.a[0], FC.b[0]); A_MFMA_ACC(acc, FC.a[1], FC.b[1]); \
            A_MFMA_ACC(acc, FC.a[2], FC.b[2]); A_MFMA_ACC(acc, FC.a[3], FC.b[3]); \
            if (storex) { char* xd_ = xf + (size_t)(((it) + s0) & 7) * 4096; \
                          if (ni & 1) { *(bf16x8*)(xd_) = FC.a[2]; *(bf16x8*)(xd_ + 1024) = FC.a[3]; } \
                          else { *(bf16x8*)(xd_) = FC.a[0]; *(bf16x8*)(xd_ + 1024) = FC.a[1]; } \
                          asm volatile("" ::: "memory"); } \
        } while (0)
        K1_STAGE(f0, f1, 0); K1_STAGE(f1, f0, 1);
        K1_RLOAD();
        K1_STAGE(f0, f1, 2); K1_STAGE(f1, f0, 3); K1_STAGE(f0, f1, 4);
        K1_REPACK();
        K1_STAGE(f1, f0, 5); K1_STAGE(f0, f1, 6); K1_STAGE(f1, f0, 7);
#undef K1_STAGE
        asm volatile("s_nop 7\n\ts_nop 7\n\ts_nop 3" : "+v"(acc));
        STAMP(ws, 0, 1);
        const int head = 2 * (2 * xj + (ni & 1)) + (r >> 4), d = r & 15;
#pragma unroll
        for (int s = 0; s < 2; ++s)
            *(u32x4*)(ws + WS_V + ((size_t)head * 128 + 2 * tt + s) * 512 + h * 256 + d * 16) = pack8(acc, s);
    }
#undef K1_ADDR
#undef K1_RLOAD
#undef K1_REPACK
    STAMP(ws, 0, 2);
}

#define X_NOLDS 0
#define X_NOMLP 0
constexpr int K2_CONST = 128 * 1024;
constexpr int K2_RED = K2_CONST + 20 * 512;
constexpr int K2_FLAG = K2_RED + 16384;
constexpr int K2_LDS = K2_FLAG + 64;
constexpr float K2_LIM = 65536.f;
constexpr int K2_PF = 8;

#define A_MFMA_S(D, K) asm volatile("v_mfma_f32_32x32x16_bf16 %0, %1, %2, %3" : "=&v"(D) : "v"(K), "v"(qf), "v"(negm) : "memory")
#define A_MFMA_PV(V, P) asm volatile("v_mfma_f32_32x32x16_bf16 %0, %1, %2, %0" : "+v"(og) : "v"(V), "v"(P))
#define A_MFMA_PV0(V, P) asm volatile("v_mfma_f32_32x32x16_bf16 %0, %1, %2, 0" : "=&v"(og) : "v"(V), "v"(P))
#define A_EXP4(E, S, i) asm volatile("v_exp_f32 %0, %4\n\tv_exp_f32 %1, %5\n\tv_exp_f32 %2, %6\n\tv_exp_f32 %3, %7" \
        : "=&v"(E[i]), "=&v"(E[i + 1]), "=&v"(E[i + 2]), "=&v"(E[i + 3]) : "v"(S[i]), "v"(S[i + 1]), "v"(S[i + 2]), "v"(S[i + 3]))
#define A_CVT4(P, E, i) asm volatile("v_cvt_pk_bf16_f32 %0, %4, %5\n\tv_cvt_pk_bf16_f32 %1, %6, %7\n\tv_cvt_pk_bf16_f32 %2, %8, %9\n\tv_cvt_pk_bf16_f32 %3, %10, %11" \
        : "=&v"(P[0]), "=&v"(P[1]), "=&v"(P[2]), "=&v"(P[3]) \
        : "v"(E[i]), "v"(E[i + 1]), "v"(E[i + 2]), "v"(E[i + 3]), "v"(E[i + 4]), "v"(E[i + 5]), "v"(E[i + 6]), "v"(E[i + 7]))

__global__ __launch_bounds__(768) void k2_kernel(char* __restrict__ ws, const u16* __restrict__ b1, const u16* __restrict__ wo,
                                                 const u16* __restrict__ x, const u16* __restrict__ gw, u16* __restrict__ out) {
    extern __shared__ __attribute__((aligned(16))) char smem[];
    LAS char* lds = (LAS char*)smem;
    const int tid = threadIdx.x, lane = tid & 63;
    const int wid = __builtin_amdgcn_readfirstlane(tid >> 6);
    const int bid = blockIdx.x;
    const int r = lane & 31, h = lane >> 5;
    STAMPL(0);

    const int head = ((bid >> 1) & 3) * 8 + ((bid >> 3) & 7), qb = (bid & 1) * 4 + (bid >> 6);
    const int qt = qb * 8 + (wid & 7);
    const int c0 = (bid & 1) * 4;
    bf16x8 qf;

    const char* kg = ws + WS_K + (size_t)head * 65536 + (wid & 7) * 1024;
    const char* vg = ws + WS_V + (size_t)head * 65536 + (wid & 7) * 1024;
    const unsigned ldsw = (unsigned)(uintptr_t)lds + (wid & 7) * 1024;
#define K2_CHUNK(c) do { const int cs_ = (((c) + c0) & 7) * 8192; \
        glds16(kg + cs_, lane * 16, ldsw + (c) * 8192); glds16(vg + cs_, lane * 16, ldsw + 65536 + (c) * 8192); } while (0)
    if (wid < 8) {
        asm volatile("global_load_dwordx4 %0, %1, %2" : "=v"(qf) : "v"(lane * 16), "s"(ws + WS_Q + ((size_t)head * 64 + qt) * 1024) : "memory");
        K2_CHUNK(0); K2_CHUNK(1); K2_CHUNK(2); K2_CHUNK(3);
    }
    if (tid < 4) { *(LAS u32x4*)(lds + K2_FLAG + tid * 16) = (u32x4){0u, 0u, 0u, 0u}; }
    asm volatile("s_waitcnt lgkmcnt(0)" ::: "memory");
    BAR();

    if (wid >= 8) {
        const int tb = (bid & 1) * 16 + ((bid >> 1) & 3) * 4 + ((bid >> 3) & 3), nb = bid >> 5;
        const int ni = wid & 1, kh = (wid >> 1) & 1;
        __builtin_amdgcn_s_setprio(3);
        const unsigned vo = lane * 16;
        volatile LAS unsigned* lw = (volatile LAS unsigned*)(lds + K2_FLAG);
        while (lw[9] < 8u) __builtin_amdgcn_s_sleep(4);
#define M_LOAD(D, sb) asm volatile("global_load_dwordx4 %0, %1, %2" : "=v"(D) : "v"(vo), "s"(sb) : "memory")
#define M_LOAD_SC1(D, sb) asm volatile("global_load_dwordx4 %0, %1, %2 sc1" : "=v"(D) : "v"(vo), "s"(sb) : "memory")
#define M_GAP() asm volatile("s_nop 7\n\ts_nop 3")
#define M_MFMA0(C, A, B) asm volatile("v_mfma_f32_32x32x16_bf16 %0, %1, %2, 0" : "=&v"(C) : "v"(A), "v"(B))
        const char* wf = ws + WS_W2 + ((size_t)(nb * 2 + ni) * NKS_F + kh * 43) * 1024;
        const char* af = ws + WS_ACT + ((size_t)(tb * 2) * NKS_F + kh * 43) * 1024;
        const int k2o = (nb * 5 + (tb & 3) * 11) % 43;
        bf16x8 fw[K2_PF], fa0[K2_PF], fa1[K2_PF];
        {
            const int wv = nb * 4 + (wid & 3), j = wv & 1, slot = wv >> 1;
            const int ft0 = slot < 11 ? 3 * slot : 33 + 2 * (slot - 11);
            const int ft2 = slot < 11 ? ft0 + 2 : ft0 + 1;
            const char* xb = ws + WS_XF + (size_t)(tb * 2 + j) * (NKS_D * 1024);
            const char* wb0 = ws + WS_W1 + (size_t)ft0 * (NKS_D * 1024);
            const char* wb2 = ws + WS_W1 + (size_t)ft2 * (NKS_D * 1024);
            const int k1o = ((tb & 3) * 2 + j) * 4;
            bf16x8 fx[6], f0[6], f1[6], f2[6];
            int kq1 = k1o;
#define P1_ISSUE(i) do { const int kk_ = kq1 * 1024; kq1 = (kq1 + 1) & 31; \
                M_LOAD(fx[i], xb + kk_); M_LOAD(f0[i], wb0 + kk_); M_LOAD(f1[i], wb0 + NKS_D * 1024 + kk_); M_LOAD(f2[i], wb2 + kk_); } while (0)
#define P1_STEP(i, n) do { asm volatile("s_waitcnt vmcnt(" #n ")" : "+v"(fx[i]), "+v"(f0[i]), "+v"(f1[i]), "+v"(f2[i]) :: "memory"); \
                A_MFMA_ACC(c[0], f0[i], fx[i]); M_GAP(); A_MFMA_ACC(c[1], f1[i], fx[i]); M_GAP(); A_MFMA_ACC(c[2], f2[i], fx[i]); } while (0)
            P1_ISSUE(0); P1_ISSUE(1); P1_ISSUE(2); P1_ISSUE(3); P1_ISSUE(4); P1_ISSUE(5);
            f32x16 c[3];
#pragma unroll
            for (int t = 0; t < 3; ++t) {
                const unsigned* bs = (const unsigned*)(b1 + 32 * (t < 2 ? ft0 + t : ft2));
#pragma unroll
                for (int e = 0; e < 8; ++e) {
                    const int d0 = (e & 1) + 4 * (e >> 1);
                    const unsigned bw = h ? bs[d0 + 2] : bs[d0];
                    c[t][2 * e] = __uint_as_float(bw << 16); c[t][2 * e + 1] = __uint_as_float(bw & 0xffff0000u);
                }
            }
            asm volatile("s_nop 1" : "+v"(c[0]), "+v"(c[1]), "+v"(c[2]));
            for (int rd = 0; rd < 4; ++rd) {
                P1_STEP(0, 20); P1_ISSUE(0); P1_STEP(1, 20); P1_ISSUE(1); P1_STEP(2, 20); P1_ISSUE(2);
                P1_STEP(3, 20); P1_ISSUE(3); P1_STEP(4, 20); P1_ISSUE(4); P1_STEP(5, 20); P1_ISSUE(5);
            }
            P1_STEP(0, 20); P1_ISSUE(0); P1_STEP(1, 20); P1_ISSUE(1);
            P1_STEP(2, 20); P1_STEP(3, 16); P1_STEP(4, 12); P1_STEP(5, 8); P1_STEP(0, 4); P1_STEP(1, 0);
#undef P1_ISSUE
#undef P1_STEP
            {
                int kk = k2o;
#pragma unroll
                for (int k = 0; k < K2_PF; ++k) { M_LOAD(fw[k], wf + kk * 1024); kk = kk == 42 ? 0 : kk + 1; }
            }
            u32x4 gx0, gx1, gg;
            M_LOAD(gx0, x + (size_t)(tb * 64 + wv * 2) * D_); M_LOAD(gx1, x + (size_t)(tb * 64 + wv * 2 + 1) * D_); M_LOAD(gg, gw);
            asm volatile("s_nop 7\n\ts_nop 7\n\ts_nop 3" : "+v"(c[0]), "+v"(c[1]), "+v"(c[2]));
            STAMPL(4);
#pragma unroll
            for (int t = 0; t < 3; ++t) {
                const int ft = t < 2 ? ft0 + t : ft2;
                f32x16 a = c[t];
#pragma unroll
                for (int e = 0; e < 16; ++e) a[e] = mish_f(a[e]);
                const char* dst = ws + WS_ACT + ((size_t)(tb * 2 + j) * NKS_F + 2 * ft) * 1024;
                const u32x4 p0 = pack8(a, 0), p1 = pack8(a, 1);
                asm volatile("global_store_dwordx4 %0, %1, %2 sc1\n\ts_nop 1" :: "v"(vo), "v"(p0), "s"(dst) : "memory");
                asm volatile("global_store_dwordx4 %0, %1, %2 offset:1024 sc1\n\ts_nop 1" :: "v"(vo), "v"(p1), "s"(dst) : "memory");
            }
            {
                asm volatile("s_waitcnt vmcnt(6)" : "+v"(gx0), "+v"(gx1), "+v"(gg) :: "memory");
                float s0_ = 0.f, s1_ = 0.f;
#pragma unroll
                for (int e = 0; e < 4; ++e) {
                    const float gl = __uint_as_float(gg[e] << 16), gh = __uint_as_float(gg[e] & 0xffff0000u);
                    s0_ += __uint_as_float(gx0[e] << 16) * gl + __uint_as_float(gx0[e] & 0xffff0000u) * gh;
                    s1_ += __uint_as_float(gx1[e] << 16) * gl + __uint_as_float(gx1[e] & 0xffff0000u) * gh;
                }
                float sv = h ? s1_ : s0_;
                sv += __shfl_xor(h ? s0_ : s1_, 32);
#pragma unroll
                for (int o = 16; o > 0; o >>= 1) sv += __shfl_xor(sv, o);
                if (r == 0) __hip_atomic_store((GAS float*)(ws + WS_G) + tb * 64 + wv * 2 + h, 1.f / (1.f + __expf(-sv)), __ATOMIC_RELAXED, __HIP_MEMORY_SCOPE_AGENT);
            }
            asm volatile("s_waitcnt vmcnt(0)" : "+v"(fw[0]), "+v"(fw[1]), "+v"(fw[2]), "+v"(fw[3]), "+v"(fw[4]), "+v"(fw[5]), "+v"(fw[6]), "+v"(fw[7]) :: "memory");
            STAMPL(5);
            unsigned old = 0;
            if (lane == 0) old = __hip_atomic_fetch_add((LAS unsigned*)(lw + 4), 1u, __ATOMIC_RELAXED, __HIP_MEMORY_SCOPE_WORKGROUP);
            const bool last = __builtin_amdgcn_readfirstlane(old) == 3u;
            GAS unsigned* cnt = (GAS unsigned*)(ws + WS_CNT) + tb * 64;
            if (last && lane == 0) __hip_atomic_fetch_add(cnt, 1u, __ATOMIC_RELAXED, __HIP_MEMORY_SCOPE_AGENT);
            if (last) {
                for (unsigned spins = 0; spins < (1u << 22); ++spins) {
                    if ((unsigned)__builtin_amdgcn_readfirstlane(__hip_atomic_load(cnt, __ATOMIC_RELAXED, __HIP_MEMORY_SCOPE_AGENT)) >= 8u) break;
                    __builtin_amdgcn_s_sleep(4);
                }
                asm volatile("s_waitcnt vmcnt(0)" ::: "memory");
                if (lane == 0) lw[5] = 1u;
            } else {
                while (lw[5] == 0u) __builtin_amdgcn_s_sleep(2);
            }
            asm volatile("s_waitcnt vmcnt(0) lgkmcnt(0)" ::: "memory");
            STAMPL(6);
        }
        STAMPL(1);
        f32x16 c0, c1;
        int kq2 = k2o;
#pragma unroll
        for (int k = 0; k < K2_PF; ++k) { M_LOAD_SC1(fa0[k], af + kq2 * 1024); M_LOAD_SC1(fa1[k], af + (NKS_F + kq2) * 1024); kq2 = kq2 == 42 ? 0 : kq2 + 1; }
#define P2_ISSUE(i) do { const int kk_ = kq2 * 1024; kq2 = kq2 == 42 ? 0 : kq2 + 1; \
            M_LOAD(fw[i], wf + kk_); M_LOAD_SC1(fa0[i], af + kk_); M_LOAD_SC1(fa1[i], af + NKS_F * 1024 + kk_); } while (0)
#define P2_WAIT(i, n) asm volatile("s_waitcnt vmcnt(" #n ")" : "+v"(fw[i]), "+v"(fa0[i]), "+v"(fa1[i]) :: "memory")
#define P2_STEP(i, n) do { P2_WAIT(i, n); A_MFMA_ACC(c0, fw[i], fa0[i]); M_GAP(); A_MFMA_ACC(c1, fw[i], fa1[i]); } while (0)
        P2_WAIT(0, 14); M_MFMA0(c0, fw[0], fa0[0]); M_MFMA0(c1, fw[0], fa1[0]); P2_ISSUE(0);
        P2_STEP(1, 15); P2_ISSUE(1); P2_STEP(2, 16); P2_ISSUE(2); P2_STEP(3, 17); P2_ISSUE(3);
        P2_STEP(4, 18); P2_ISSUE(4); P2_STEP(5, 19); P2_ISSUE(5); P2_STEP(6, 20); P2_ISSUE(6); P2_STEP(7, 21); P2_ISSUE(7);
        for (int rd = 0; rd < 3; ++rd) {
            P2_STEP(0, 21); P2_ISSUE(0); P2_STEP(1, 21); P2_ISSUE(1); P2_STEP(2, 21); P2_ISSUE(2); P2_STEP(3, 21); P2_ISSUE(3);
            P2_STEP(4, 21); P2_ISSUE(4); P2_STEP(5, 21); P2_ISSUE(5); P2_STEP(6, 21); P2_ISSUE(6); P2_STEP(7, 21); P2_ISSUE(7);
        }
        P2_STEP(0, 21); P2_ISSUE(0); P2_STEP(1, 21); P2_ISSUE(1); P2_STEP(2, 21); P2_ISSUE(2);
        P2_STEP(3, 21); P2_STEP(4, 18); P2_STEP(5, 15); P2_STEP(6, 12); P2_STEP(7, 9); P2_STEP(0, 6); P2_STEP(1, 3); P2_STEP(2, 0);
#undef P2_ISSUE
#undef P2_WAIT
#undef P2_STEP
        asm volatile("s_nop 7\n\ts_nop 7\n\ts_nop 3" : "+v"(c0), "+v"(c1));
        LAS f32x4* red = (LAS f32x4*)(lds + K2_RED + ni * 8192);
        if (kh == 1) {
#pragma unroll
            for (int qd = 0; qd < 4; ++qd) {
                red[qd * 64 + lane] = (f32x4){c0[4 * qd], c0[4 * qd + 1], c0[4 * qd + 2], c0[4 * qd + 3]};
                red[(4 + qd) * 64 + lane] = (f32x4){c1[4 * qd], c1[4 * qd + 1], c1[4 * qd + 2], c1[4 * qd + 3]};
            }
            asm volatile("s_waitcnt lgkmcnt(0)" ::: "memory");
            if (lane == 0) lw[ni] = 1u;
        } else {
            while (lw[ni] == 0u) __builtin_amdgcn_s_sleep(2);
            asm volatile("" ::: "memory");
#pragma unroll
            for (int qd = 0; qd < 4; ++qd) {
                red[qd * 64 + lane] += (f32x4){c0[4 * qd], c0[4 * qd + 1], c0[4 * qd + 2], c0[4 * qd + 3]};
                red[(4 + qd) * 64 + lane] += (f32x4){c1[4 * qd], c1[4 * qd + 1], c1[4 * qd + 2], c1[4 * qd + 3]};
            }
            asm volatile("s_waitcnt lgkmcnt(0)" ::: "memory");
            if (lane == 0) __hip_atomic_fetch_add((LAS unsigned*)(lw + 2), 1u, __ATOMIC_RELAXED, __HIP_MEMORY_SCOPE_WORKGROUP);
        }
        STAMPL(7);
        const int w4 = wid & 3;
        while (lw[11] < 8u) __builtin_amdgcn_s_sleep(8);
        asm volatile("" ::: "memory");
        {
            const char* wsrc = (const char*)(wo + (size_t)(128 * w4) * D_ + nb * 64);
            const unsigned wvo = ((lane >> 2) * D_ + (lane & 3) * 8) * 2;
            const unsigned wdst = (unsigned)(uintptr_t)lds + w4 * 8192;
#pragma unroll
            for (int k = 0; k < 8; ++k) {
                glds16(wsrc + k * (16 * D_ * 2), wvo, wdst + k * 1024);
                glds16(wsrc + k * (16 * D_ * 2) + 64, wvo, wdst + 65536 + k * 1024);
            }
        }
        float g0, g1;
        asm volatile("global_load_dword %0, %2, %3 sc1\n\tglobal_load_dword %1, %2, %3 offset:128 sc1"
                     : "=&v"(g0), "=&v"(g1) : "v"(r * 4), "s"(ws + WS_G + tb * 256) : "memory");
        if (w4 == 0) {
            GAS unsigned* oc = (GAS unsigned*)(ws + WS_CNT) + (32 + (tb >> 2)) * 64;
            for (unsigned spins = 0; spins < (1u << 22); ++spins) {
                if ((unsigned)__builtin_amdgcn_readfirstlane(__hip_atomic_load(oc, __ATOMIC_RELAXED, __HIP_MEMORY_SCOPE_AGENT)) >= 32u) break;
                __builtin_amdgcn_s_sleep(4);
            }
            asm volatile("s_waitcnt vmcnt(0)" ::: "memory");
            if (lane == 0) lw[6] = 1u;
        } else {
            while (lw[6] == 0u) __builtin_amdgcn_s_sleep(2);
        }
        asm volatile("s_waitcnt vmcnt(0) lgkmcnt(0)" : "+v"(g0), "+v"(g1) :: "memory");
        bf16x8 fo0[8], fo1[8];
        {
            const char* ob = ws + WS_O + ((size_t)(tb * 2) * NKS_D + w4 * 8) * 1024;
#pragma unroll
            for (int k = 0; k < 8; ++k) { M_LOAD_SC1(fo0[k], ob + k * 1024); M_LOAD_SC1(fo1[k], ob + (NKS_D + k) * 1024); }
        }
        f32x16 fa[4];
        {
            const LAS char* rb = lds + w4 * 8192 + (4 * h + ((lane & 15) >> 2)) * 64 + ((lane >> 4) & 1) * 32 + (lane & 3) * 8;
#define F_STEP(k, n) do { \
                v4i16_t l0 = __builtin_amdgcn_ds_read_tr16_b64_v4i16((LAS v4i16_t*)(rb + (k) * 1024)); \
                v4i16_t h0 = __builtin_amdgcn_ds_read_tr16_b64_v4i16((LAS v4i16_t*)(rb + (k) * 1024 + 512)); \
                v4i16_t l1 = __builtin_amdgcn_ds_read_tr16_b64_v4i16((LAS v4i16_t*)(rb + 65536 + (k) * 1024)); \
                v4i16_t h1 = __builtin_amdgcn_ds_read_tr16_b64_v4i16((LAS v4i16_t*)(rb + 65536 + (k) * 1024 + 512)); \
                const bf16x8 w0_ = __builtin_bit_cast(bf16x8, __builtin_shufflevector(l0, h0, 0, 1, 2, 3, 4, 5, 6, 7)); \
                const bf16x8 w1_ = __builtin_bit_cast(bf16x8, __builtin_shufflevector(l1, h1, 0, 1, 2, 3, 4, 5, 6, 7)); \
                asm volatile("s_waitcnt vmcnt(" #n ")" : "+v"(fo0[k]), "+v"(fo1[k]) :: "memory"); \
                if ((k) == 0) { M_MFMA0(fa[0], w0_, fo0[k]); M_MFMA0(fa[1], w0_, fo1[k]); M_MFMA0(fa[2], w1_, fo0[k]); M_MFMA0(fa[3], w1_, fo1[k]); } \
                else { A_MFMA_ACC(fa[0], w0_, fo0[k]); A_MFMA_ACC(fa[1], w0_, fo1[k]); A_MFMA_ACC(fa[2], w1_, fo0[k]); A_MFMA_ACC(fa[3], w1_, fo1[k]); } \
            } while (0)
            F_STEP(0, 14); F_STEP(1, 12); F_STEP(2, 10); F_STEP(3, 8); F_STEP(4, 6); F_STEP(5, 4); F_STEP(6, 2); F_STEP(7, 0);
#undef F_STEP
        }
        asm volatile("s_nop 7\n\ts_nop 7\n\ts_nop 3" : "+v"(fa[0]), "+v"(fa[1]), "+v"(fa[2]), "+v"(fa[3]));
        while (lw[7] < 8u) __builtin_amdgcn_s_sleep(2);
        asm volatile("" ::: "memory");
        LAS f32x4* fr = (LAS f32x4*)(lds + 32768);
#define F_SLOT(w, t, qd) ((((w) & 1) * 16 + (t) * 4 + (qd)) * 64 + ((w) >> 1) * 4096 + lane)
#pragma unroll
        for (int t = 0; t < 4; ++t)
#pragma unroll
            for (int qd = 0; qd < 4; ++qd)
                fr[F_SLOT(w4, t, qd)] = (f32x4){fa[t][4 * qd], fa[t][4 * qd + 1], fa[t][4 * qd + 2], fa[t][4 * qd + 3]};
        asm volatile("s_waitcnt lgkmcnt(0)" ::: "memory");
        if (lane == 0) __hip_atomic_fetch_add((LAS unsigned*)(lw + 3), 1u, __ATOMIC_RELAXED, __HIP_MEMORY_SCOPE_WORKGROUP);
        while (lw[3] < 4u || lw[2] < 2u) __builtin_amdgcn_s_sleep(1);
        asm volatile("" ::: "memory");
        {
            const float g = (w4 & 1) ? g1 : g0;
            const LAS f32x4* bk = (const LAS f32x4*)(lds + K2_RED) + (size_t)w4 * 256 + lane;
            u16* op = out + (size_t)(tb * 64 + (w4 & 1) * 32 + r) * D_ + nb * 64 + (w4 >> 1) * 32 + 4 * h;
#pragma unroll
            for (int qd = 0; qd < 4; ++qd) {
                f32x4 sa = fr[F_SLOT(0, w4, qd)] + fr[F_SLOT(1, w4, qd)] + fr[F_SLOT(2, w4, qd)] + fr[F_SLOT(3, w4, qd)];
                sa = sa * g + bk[qd * 64] * (1.f - g);
                *(u32x2*)(op + 8 * qd) = (u32x2){pk2(sa[0], sa[1]), pk2(sa[2], sa[3])};
            }
        }
#undef F_SLOT
        STAMPL(3);
        STAMPL_FLUSH(ws, 1);
        return;
    }

    if (tid < 40) {
        const unsigned c = (tid & 1) ? 0u : 0x3f803f80u;
        *(LAS u32x4*)(lds + K2_CONST + (tid >> 1) * 512 + (tid & 1) * 16) = (u32x4){c, c, c, c};
    }
    const bool vact = r < 16;
    const LAS char* kp = lds + lane * 16;
    const LAS char* vp = vact ? lds + 65536 + h * 256 + r * 16 : lds + K2_CONST + (r == 16 ? 0 : 16);
    const int vinc = vact ? 8192 : 0;

    f32x16 ot = (f32x16){};
    f32x16 negm, sa, sb;
    bf16x8 ka, kb, va0, va1, vb0, vb1;
    if (X_NOLDS) { vb0 = *(const LAS bf16x8*)(vp); vb1 = *(const LAS bf16x8*)(vp + 512); }
    float m;
    volatile LAS unsigned* cw = (volatile LAS unsigned*)(lds + K2_FLAG) + 8;
#define K2_SIGNAL(c) do { if (lane == 0) __hip_atomic_fetch_add((LAS unsigned*)(cw + (c)), 1u, __ATOMIC_RELAXED, __HIP_MEMORY_SCOPE_WORKGROUP); } while (0)
#define K2_NEED(c) do { while (cw[c] < 8u) __builtin_amdgcn_s_sleep(1); asm volatile("" ::: "memory"); } while (0)
    asm volatile("s_waitcnt vmcnt(2) lgkmcnt(0)" : "+v"(qf) :: "memory");
    K2_CHUNK(4); K2_CHUNK(5); K2_CHUNK(6); K2_CHUNK(7);
    K2_SIGNAL(1);
    K2_NEED(1);
    STAMPL(1);
    {
        ka = *(const LAS bf16x8*)kp;
        kb = *(const LAS bf16x8*)(kp + 1024);
        va0 = *(const LAS bf16x8*)(vp); va1 = *(const LAS bf16x8*)(vp + 512);
        f32x16 s = MFMA(ka, qf, (f32x16){});
        float mx = fmaxf(s[0], s[1]);
#pragma unroll
        for (int e = 2; e < 16; ++e) mx = fmaxf(mx, s[e]);
        mx = fmaxf(mx, __shfl_xor(mx, 32));
        m = mx;
#pragma unroll
        for (int e = 0; e < 16; ++e) negm[e] = -m;
        asm volatile("s_nop 3" : "+v"(negm));
        A_MFMA_S(sa, ka);
        asm volatile("s_nop 7\n\ts_nop 7" : "+v"(sa));
    }
    const LAS char* kq = kp;
    const LAS char* vq = vp;
    int g = 0;
    for (; g < 8; ++g) {
        if (g == 1) { WAIT_V(0); K2_SIGNAL(2); }
        else if (g == 2) K2_NEED(2);
        else if (g == 5) K2_SIGNAL(3);
        if (g == 0) __builtin_amdgcn_s_setprio(2);
        else if (g == 5) __builtin_amdgcn_s_setprio(1);
        else if (g == 7) __builtin_amdgcn_s_setprio(0);
        const LAS char* kq2 = (g == 7) ? kp - 8192 : kq;
        const LAS char* vq2 = (g == 7) ? vp - vinc : vq;
        asm volatile("" : "+v"(kq), "+v"(vq), "+v"(kq2), "+v"(vq2));
        f32x16 og;
        float e[16];
        u32x4 p0, p1;
#define K2_TILE(SC, SN, KC, KN, VC0, VP1, VN0, VN1, t) do { \
            if ((t) > 0) A_MFMA_PV(VP1, p1); \
            if (!X_NOLDS) { \
            KN = ((t) + 2 < 8) ? *(const LAS bf16x8*)(kq + ((t) + 2) * 1024) : *(const LAS bf16x8*)(kq2 + ((t) + 2) * 1024); \
            if ((t) + 1 < 8) { VN0 = *(const LAS bf16x8*)(vq + ((t) + 1) * 1024); VN1 = *(const LAS bf16x8*)(vq + ((t) + 1) * 1024 + 512); } \
            else { VN0 = *(const LAS bf16x8*)(vq2 + ((t) + 1) * 1024); VN1 = *(const LAS bf16x8*)(vq2 + ((t) + 1) * 1024 + 512); } } \
            A_EXP4(e, SC, 0); A_EXP4(e, SC, 4); \
            A_MFMA_S(SN, KC); \
            A_CVT4(p0, e, 0); \
            A_EXP4(e, SC, 8); \
            if ((t) == 0) A_MFMA_PV0(VC0, p0); else A_MFMA_PV(VC0, p0); \
            A_EXP4(e, SC, 12); \
            A_CVT4(p1, e, 8); \
            asm volatile("s_nop 1"); \
        } while (0)
#pragma unroll
        for (int t = 0; t < 8; t += 2) {
            K2_TILE(sa, sb, kb, ka, va0, vb1, vb0, vb1, t);
            K2_TILE(sb, sa, ka, kb, vb0, va1, va0, va1, t + 1);
        }
        A_MFMA_PV(vb1, p1);
        asm volatile("s_nop 7\n\ts_nop 7\n\ts_nop 3" : "+v"(og));
#undef K2_TILE
        if (__builtin_expect(__any(!(og[8] <= K2_LIM)), 0)) break;
#pragma unroll
        for (int i = 0; i < 9; ++i) ot[i] += og[i];
        kq += 8192; vq += vinc;
    }
    if (__builtin_expect(g < 8, 0)) {
        WAIT_V(0);
        if (g < 1) K2_SIGNAL(2);
        K2_NEED(2);
        for (int t = g * 8; t < 64; ++t) {
            bf16x8 kf = *(const LAS bf16x8*)(kp + t * 1024);
            const LAS char* vt = vp + (vact ? t * 1024 : 0);
            vb0 = *(const LAS bf16x8*)(vt); vb1 = *(const LAS bf16x8*)(vt + 512);
            f32x16 s = MFMA(kf, qf, (f32x16){});
            float mx = fmaxf(s[0], s[1]);
#pragma unroll
            for (int i = 2; i < 16; ++i) mx = fmaxf(mx, s[i]);
            mx = fmaxf(mx, __shfl_xor(mx, 32));
            float mn = fmaxf(m, mx);
            float al = __builtin_amdgcn_exp2f(m - mn);
            m = mn;
            ot = ot * al;
#pragma unroll
            for (int i = 0; i < 16; ++i) s[i] = __builtin_amdgcn_exp2f(s[i] - m);
            ot = MFMA(vb0, __builtin_bit_cast(bf16x8, pack8(s, 0)), ot);
            ot = MFMA(vb1, __builtin_bit_cast(bf16x8, pack8(s, 1)), ot);
        }
        asm volatile("s_waitcnt lgkmcnt(0)" ::: "memory");
        if (g < 5) K2_SIGNAL(3);
    }
    STAMPL(2);
    float l = __shfl(ot[8], r);
    float rl = 1.f / l;
    f32x16 on = ot * rl;
    {
        const u32x4 po = pack8(on, 0);
        asm volatile("global_store_dwordx4 %0, %1, %2 sc1\n\ts_nop 1\n\ts_waitcnt vmcnt(0)"
                     :: "v"(lane * 16), "v"(po), "s"(ws + WS_O + ((size_t)qt * NKS_D + head) * 1024) : "memory");
        unsigned old = 0;
        if (lane == 0) old = __hip_atomic_fetch_add((LAS unsigned*)(cw - 1), 1u, __ATOMIC_RELAXED, __HIP_MEMORY_SCOPE_WORKGROUP);
        if (__builtin_amdgcn_readfirstlane(old) == 7u && lane == 0)
            __hip_atomic_fetch_add((GAS unsigned*)(ws + WS_CNT) + (32 + qb) * 64, 1u, __ATOMIC_RELAXED, __HIP_MEMORY_SCOPE_AGENT);
    }
    STAMPL(3);
    STAMPL_FLUSH(ws, 1);
}

extern "C" void kernel_launch(void* const* d_in, const int* in_sizes, int n_in,
                              void* d_out, int out_size, void* d_ws, size_t ws_size,
                              hipStream_t stream) {
    const u16* x = (const u16*)d_in[0];
    const u16* wqkv = (const u16*)d_in[1];
    const u16* wo = (const u16*)d_in[2];
    const u16* w1 = (const u16*)d_in[4];
    const u16* b1 = (const u16*)d_in[5];
    const u16* w2 = (const u16*)d_in[6];
    const u16* gw = (const u16*)d_in[7];
    char* ws = (char*)d_ws;
    (void)hipFuncSetAttribute((const void*)k1_kernel, hipFuncAttributeMaxDynamicSharedMemorySize, K1_LDS);
    (void)hipFuncSetAttribute((const void*)k2_kernel, hipFuncAttributeMaxDynamicSharedMemorySize, K2_LDS);
    k1_kernel<<<256, 1024, K1_LDS, stream>>>(x, wqkv, w1, w2, ws);
    k2_kernel<<<256, 768, K2_LDS, stream>>>(ws, b1, wo, x, gw, (u16*)d_out);
}
```

```cpp
#include <hip/hip_runtime.h>
#include <hip/hip_bf16.h>
#include <stdint.h>

typedef __attribute__((ext_vector_type(8))) __bf16 bf16x8;
typedef __attribute__((ext_vector_type(2))) __bf16 bf16x2;
typedef __attribute__((ext_vector_type(2))) float f32x2;
typedef __attribute__((ext_vector_type(4))) float f32x4;
typedef __attribute__((ext_vector_type(16))) float f32x16;
typedef __attribute__((ext_vector_type(4))) unsigned u32x4;
typedef __attribute__((ext_vector_type(2))) unsigned u32x2;
typedef short v4i16_t __attribute__((ext_vector_type(4)));
typedef short v8i16_t __attribute__((ext_vector_type(8)));
typedef unsigned short u16;
#define LAS __attribute__((address_space(3)))
#define GAS __attribute__((address_space(1)))
#define DI __device__ __forceinline__
#define MFMA(a, b, c) __builtin_amdgcn_mfma_f32_32x32x16_bf16((a), (b), (c), 0, 0, 0)

constexpr int S_ = 2048, D_ = 512, H_ = 32, F_ = 1376;
constexpr int NKS_F = F_ / 16;
constexpr int NKS_D = D_ / 16;

constexpr size_t WS_Q = 0;
constexpr size_t WS_K = WS_Q + (size_t)2 * 1024 * 1024;
constexpr size_t WS_V = WS_K + (size_t)2 * 1024 * 1024;
constexpr size_t WS_O = WS_V + (size_t)2 * 1024 * 1024;
constexpr size_t WS_ACT = WS_O + (size_t)2 * 1024 * 1024;
constexpr size_t WS_W2 = WS_ACT + (size_t)64 * NKS_F * 1024;
constexpr size_t WS_W1 = WS_W2 + (size_t)16 * NKS_F * 1024;
constexpr size_t WS_XF = WS_W1 + (size_t)43 * NKS_D * 1024;
constexpr size_t WS_CNT = WS_XF + (size_t)64 * NKS_D * 1024;
constexpr size_t WS_G = WS_CNT + 64 * 256;

DI unsigned pk2(float a, float b) { bf16x2 r = __builtin_convertvector((f32x2){a, b}, bf16x2); return __builtin_bit_cast(unsigned, r); }
DI float bf2f(u16 b) { return __uint_as_float((unsigned)b << 16); }
DI int crow(int r, int h) { return (r & 3) + 8 * (r >> 2) + 4 * h; }
DI u32x4 pack8(const f32x16& x, int s) {
    u32x4 p;
    p[0] = pk2(x[8 * s + 0], x[8 * s + 1]); p[1] = pk2(x[8 * s + 2], x[8 * s + 3]);
    p[2] = pk2(x[8 * s + 4], x[8 * s + 5]); p[3] = pk2(x[8 * s + 6], x[8 * s + 7]);
    return p;
}
DI void glds16(const char* sbase, unsigned voff, unsigned lds_base) {
    unsigned sv;
    asm volatile("s_mov_b32 %0, m0\n\ts_mov_b32 m0, %3\n\ts_nop 0\n\tglobal_load_lds_dwordx4 %1, %2\n\ts_mov_b32 m0, %0"
                 : "=&s"(sv) : "v"(voff), "s"(sbase), "s"(lds_base) : "memory");
}
#define STAMP(ws, kid, idx)
#define STAMPL(i)
#define STAMPL_FLUSH(ws, kid)
#define WAIT_V(n) asm volatile("s_waitcnt vmcnt(" #n ")" ::: "memory")
#define BAR() __builtin_amdgcn_s_barrier()

DI float mish_f(float h) {
    const float n = __builtin_amdgcn_exp2f(fminf(h, 20.f) * 1.4426950408889634f);
    const float w = n * (n + 2.f);
    return h * w * __builtin_amdgcn_rcpf(w + 2.f);
}

constexpr int K1_SA = 64 * 64 * 2;
constexpr int K1_SB = 3 * 64 * 64 * 2;
constexpr int K1_BOFF = 8 * K1_SA;
constexpr int K1_LDS = K1_BOFF + 4 * K1_SB;
constexpr int NFRAG = 16 * NKS_F + 43 * NKS_D;

#define A_MFMA_ACC(C, A, B) asm volatile("v_mfma_f32_32x32x16_bf16 %0, %1, %2, %0" : "+v"(C) : "v"(A), "v"(B))

struct K1Frag { bf16x8 a[4], b[4]; };
DI void k1_ldfrag(K1Frag& f, const LAS char* lds, int it, const int (&aoff)[4], int boff) {
    int ao_ = it * K1_SA, bo_ = (it & 3) * K1_SB;
    asm volatile("" : "+s"(ao_), "+s"(bo_));
#pragma unroll
    for (int ks = 0; ks < 4; ++ks) {
        f.a[ks] = *(const LAS bf16x8*)(lds + ao_ + aoff[ks]);
        v4i16_t lo = __builtin_amdgcn_ds_read_tr16_b64_v4i16((LAS v4i16_t*)(lds + bo_ + boff + ks * 2048));
        v4i16_t hi = __builtin_amdgcn_ds_read_tr16_b64_v4i16((LAS v4i16_t*)(lds + bo_ + boff + ks * 2048 + 512));
        v8i16_t w = __builtin_shufflevector(lo, hi, 0, 1, 2, 3, 4, 5, 6, 7);
        f.b[ks] = __builtin_bit_cast(bf16x8, w);
    }
}

__global__ __launch_bounds__(1024) void k1_kernel(const u16* __restrict__ x, const u16* __restrict__ wqkv, const u16* __restrict__ w1,
                                                  const u16* __restrict__ w2, char* __restrict__ ws) {
    extern __shared__ __attribute__((aligned(16))) char smem[];
    LAS char* lds = (LAS char*)smem;
    const int tid = threadIdx.x, lane = tid & 63;
    const int wid = __builtin_amdgcn_readfirstlane(tid >> 6);
    const int bid = blockIdx.x;
    const int xj = (bid & 6) + (bid >> 7), mt = (bid & 1) * 16 + ((bid >> 3) & 15);
    const int r = lane & 31, h = lane >> 5;
    const int s0 = mt & 7;
    const unsigned lds0 = (unsigned)(uintptr_t)lds;
    STAMP(ws, 0, 0);

    if (wid >= 12) {
        const int lw = wid - 12;
        const int row = lw * 16 + (lane >> 3);
        const char* abase = (const char*)(x + (size_t)(mt * 64) * D_);
        const char* bbase = (const char*)(wqkv + 64 * xj);
        const unsigned ao0 = (row * D_ + (((lane & 7) ^ ((row >> 1) & 7)) << 3)) * 2;
        const unsigned ao1 = ((row + 8) * D_ + (((lane & 7) ^ (((row + 8) >> 1) & 7)) << 3)) * 2;
        const unsigned bo0 = (row * (3 * D_) + (((lane & 7) ^ (((row >> 1) & 1) << 2)) << 3)) * 2;
        const unsigned bo1 = bo0 + 8 * (3 * D_) * 2;
        const unsigned adst = lds0 + lw * 2048, bdst = lds0 + K1_BOFF + lw * 2048;
#define K1_ISSUE_A(it) do { const int st_ = ((it) + s0) & 7; \
            glds16(abase + st_ * 128, ao0, adst + (it) * K1_SA); glds16(abase + st_ * 128, ao1, adst + (it) * K1_SA + 1024); } while (0)
#define K1_ISSUE_B(it) do { const int st_ = ((it) + s0) & 7; const int bf_ = ((it) & 3) * K1_SB; \
            const char* b_ = bbase + (size_t)st_ * (128 * 3 * D_); \
            _Pragma("unroll") for (int j_ = 0; j_ < 3; ++j_) { \
                glds16(b_ + j_ * (2 * D_), bo0, bdst + bf_ + j_ * 8192); glds16(b_ + j_ * (2 * D_), bo1, bdst + bf_ + j_ * 8192 + 1024); } } while (0)
        K1_ISSUE_A(0); K1_ISSUE_B(0); K1_ISSUE_A(1); K1_ISSUE_B(1);
        WAIT_V(8);  BAR(); K1_ISSUE_A(2); K1_ISSUE_B(2); K1_ISSUE_A(3); K1_ISSUE_B(3);
        WAIT_V(16); BAR(); K1_ISSUE_B(4); K1_ISSUE_A(4); K1_ISSUE_A(5);
        WAIT_V(18); BAR(); K1_ISSUE_B(5); K1_ISSUE_A(6); K1_ISSUE_A(7);
        WAIT_V(20); BAR(); K1_ISSUE_B(6);
        WAIT_V(18); BAR(); K1_ISSUE_B(7);
        WAIT_V(16); BAR();
        WAIT_V(6);  BAR();
        WAIT_V(0);  BAR();
#undef K1_ISSUE_A
#undef K1_ISSUE_B
        if (bid == 0 && lw == 0) ((unsigned*)(ws + WS_CNT))[lane * 64] = 0u;
        return;
    }

    const int mi = wid & 1, ni = wid >> 1;
    const int tt = mt * 2 + mi;
    const int rf = bid * 12 + wid;
    const bool nat = rf >= 16 * NKS_F;
#define K1_ADDR() int ln = lane; asm volatile("" : "+v"(ln)); \
    const int arow = mi * 32 + (ln & 31), asw = (arow >> 1) & 7; \
    int aoff[4]; \
    _Pragma("unroll") for (int ks = 0; ks < 4; ++ks) aoff[ks] = arow * 128 + (((2 * ks + (ln >> 5)) ^ asw) << 4); \
    const int boff = K1_BOFF + (ni >> 1) * 8192 + (8 * (ln >> 5) + ((ln & 15) >> 2)) * 128 + (((ni & 1) ^ ((ln & 15) >> 3)) << 6) + ((ln >> 4) & 1) * 32 + (ln & 3) * 8;
#define K1_RLOAD() do { int f = rf < NFRAG ? rf : 0; const u16* wsrc; int ld; \
        if (f < 16 * NKS_F) { wsrc = w2 + (size_t)(16 * (f % NKS_F)) * D_ + (f / NKS_F) * 32; ld = D_; } \
        else { f -= 16 * NKS_F; wsrc = w1 + (size_t)(16 * (f & 31)) * F_ + (f >> 5) * 32; ld = F_; } \
        glds16((const char*)wsrc, ((ln >> 2) * ld + (ln & 3) * 8) * 2, lds0 + wid * 1024); } while (0)
#define K1_REPACK() do { asm volatile("s_waitcnt vmcnt(0)" ::: "memory"); \
        const LAS char* rb = lds + wid * 1024 + ((nat ? 8 : 4) * (ln >> 5) + ((ln & 15) >> 2)) * 64 + ((ln >> 4) & 1) * 32 + (ln & 3) * 8; \
        v4i16_t lo = __builtin_amdgcn_ds_read_tr16_b64_v4i16((LAS v4i16_t*)(rb)); \
        v4i16_t hi = __builtin_amdgcn_ds_read_tr16_b64_v4i16((LAS v4i16_t*)(rb + (nat ? 256 : 512))); \
        v8i16_t w = __builtin_shufflevector(lo, hi, 0, 1, 2, 3, 4, 5, 6, 7); \
          \
        if (rf < NFRAG) asm volatile("global_store_dwordx4 %0, %1, %2 sc1\n\ts_nop 1" :: "v"(ln * 16), "v"(w), "s"(ws + WS_W2 + (size_t)rf * 1024) : "memory"); \
    } while (0)
    if (wid < 8) {
        K1_ADDR();
        f32x16 acc = (f32x16){};
        K1Frag f0, f1;
        BAR();
        STAMP(ws, 0, 3);
        k1_ldfrag(f0, lds, 0, aoff, boff);
#define K1_STAGE(FC, FN, it) do { \
            if ((it) < 7) { asm volatile("s_waitcnt lgkmcnt(0)" ::: "memory"); BAR(); k1_ldfrag(FN, lds, (it) + 1, aoff, boff); } \
            A_MFMA_ACC(acc, FC.b[0], FC.a[0]); A_MFMA_ACC(acc, FC.b[1], FC.a[1]); \
            A_MFMA_ACC(acc, FC.b[2], FC.a[2]); A_MFMA_ACC(acc, FC.b[3], FC.a[3]); \
        } while (0)
        K1_STAGE(f0, f1, 0); K1_STAGE(f1, f0, 1);
        K1_RLOAD();
        K1_STAGE(f0, f1, 2); K1_STAGE(f1, f0, 3); K1_STAGE(f0, f1, 4);
        K1_REPACK();
        K1_STAGE(f1, f0, 5); K1_STAGE(f0, f1, 6); K1_STAGE(f1, f0, 7);
#undef K1_STAGE
        asm volatile("s_nop 7\n\ts_nop 7\n\ts_nop 3" : "+v"(acc));
        STAMP(ws, 0, 1);
        const int ix = 2 * xj + (ni & 1);
        if (ni < 2) acc = acc * (0.25f * 1.4426950408889634f);
        char* dst = ws + (ni < 2 ? WS_Q : WS_K);
#pragma unroll
        for (int s = 0; s < 2; ++s)
            { const u32x4 pv = pack8(acc, s); asm volatile("global_store_dwordx4 %0, %1, off sc1\n\ts_nop 1" :: "v"(dst + ((size_t)(2 * ix + s) * 64 + tt) * 1024 + lane * 16), "v"(pv) : "memory"); }
    } else {
        char* xf = ws + WS_XF + ((size_t)tt * NKS_D + (ni & 1) * 2) * 1024 + lane * 16;
        K1_ADDR();
        f32x16 acc = (f32x16){};
        K1Frag f0, f1;
        BAR();
        k1_ldfrag(f0, lds, 0, aoff, boff);
#define K1_STAGE(FC, FN, it) do { \
            if ((it) < 7) { asm volatile("s_waitcnt lgkmcnt(0)" ::: "memory"); BAR(); k1_ldfrag(FN, lds, (it) + 1, aoff, boff); } \
            A_MFMA_ACC(acc, FC.a[0], FC.b[0]); A_MFMA_ACC(acc, FC.a[1], FC.b[1]); \
            A_MFMA_ACC(acc, FC.a[2], FC.b[2]); A_MFMA_ACC(acc, FC.a[3], FC.b[3]); \
            if ((((it) + s0) & 7) == xj) { char* xd_ = xf + (size_t)xj * 4096; \
                          if (ni & 1) asm volatile("global_store_dwordx4 %0, %1, off sc1\n\tglobal_store_dwordx4 %0, %2, off offset:1024 sc1\n\ts_nop 1" :: "v"(xd_), "v"(FC.a[2]), "v"(FC.a[3]) : "memory"); \
                          else asm volatile("global_store_dwordx4 %0, %1, off sc1\n\tglobal_store_dwordx4 %0, %2, off offset:1024 sc1\n\ts_nop 1" :: "v"(xd_), "v"(FC.a[0]), "v"(FC.a[1]) : "memory"); } \
        } while (0)
        K1_STAGE(f0, f1, 0); K1_STAGE(f1, f0, 1);
        K1_RLOAD();
        K1_STAGE(f0, f1, 2); K1_STAGE(f1, f0, 3); K1_STAGE(f0, f1, 4);
        K1_REPACK();
        K1_STAGE(f1, f0, 5); K1_STAGE(f0, f1, 6); K1_STAGE(f1, f0, 7);
#undef K1_STAGE
        asm volatile("s_nop 7\n\ts_nop 7\n\ts_nop 3" : "+v"(acc));
        STAMP(ws, 0, 1);
        const int head = 2 * (2 * xj + (ni & 1)) + (r >> 4), d = r & 15;
#pragma unroll
        for (int s = 0; s < 2; ++s)
            { const u32x4 pv = pack8(acc, s); asm volatile("global_store_dwordx4 %0, %1, off sc1\n\ts_nop 1" :: "v"(ws + WS_V + ((size_t)head * 128 + 2 * tt + s) * 512 + h * 256 + d * 16), "v"(pv) : "memory"); }
    }
#undef K1_ADDR
#undef K1_RLOAD
#undef K1_REPACK
    STAMP(ws, 0, 2);
}

#define X_NOLDS 0
#define X_NOMLP 0
constexpr int K2_CONST = 128 * 1024;
constexpr int K2_RED = K2_CONST + 20 * 512;
constexpr int K2_FLAG = K2_RED + 16384;
constexpr int K2_LDS = K2_FLAG + 64;
constexpr float K2_LIM = 65536.f;
constexpr int K2_PF = 8;

#define A_MFMA_S(D, K) asm volatile("v_mfma_f32_32x32x16_bf16 %0, %1, %2, %3" : "=&v"(D) : "v"(K), "v"(qf), "v"(negm) : "memory")
#define A_MFMA_PV(V, P) asm volatile("v_mfma_f32_32x32x16_bf16 %0, %1, %2, %0" : "+v"(og) : "v"(V), "v"(P))
#define A_MFMA_PV0(V, P) asm volatile("v_mfma_f32_32x32x16_bf16 %0, %1, %2, 0" : "=&v"(og) : "v"(V), "v"(P))
#define A_EXP4(E, S, i) asm volatile("v_exp_f32 %0, %4\n\tv_exp_f32 %1, %5\n\tv_exp_f32 %2, %6\n\tv_exp_f32 %3, %7" \
        : "=&v"(E[i]), "=&v"(E[i + 1]), "=&v"(E[i + 2]), "=&v"(E[i + 3]) : "v"(S[i]), "v"(S[i + 1]), "v"(S[i + 2]), "v"(S[i + 3]))
#define A_CVT4(P, E, i) asm volatile("v_cvt_pk_bf16_f32 %0, %4, %5\n\tv_cvt_pk_bf16_f32 %1, %6, %7\n\tv_cvt_pk_bf16_f32 %2, %8, %9\n\tv_cvt_pk_bf16_f32 %3, %10, %11" \
        : "=&v"(P[0]), "=&v"(P[1]), "=&v"(P[2]), "=&v"(P[3]) \
        : "v"(E[i]), "v"(E[i + 1]), "v"(E[i + 2]), "v"(E[i + 3]), "v"(E[i + 4]), "v"(E[i + 5]), "v"(E[i + 6]), "v"(E[i + 7]))

__global__ __launch_bounds__(768) void k2_kernel(char* __restrict__ ws, const u16* __restrict__ b1, const u16* __restrict__ wo,
                                                 const u16* __restrict__ x, const u16* __restrict__ gw, u16* __restrict__ out) {
    extern __shared__ __attribute__((aligned(16))) char smem[];
    LAS char* lds = (LAS char*)smem;
    const int tid = threadIdx.x, lane = tid & 63;
    const int wid = __builtin_amdgcn_readfirstlane(tid >> 6);
    const int bid = blockIdx.x;
    const int r = lane & 31, h = lane >> 5;
    STAMPL(0);

    const int head = ((bid >> 1) & 3) * 8 + ((bid >> 3) & 7), qb = (bid & 1) * 4 + (bid >> 6);
    const int qt = qb * 8 + (wid & 7);
    const int c0 = (bid & 1) * 4;
    bf16x8 qf;

    const char* kg = ws + WS_K + (size_t)head * 65536 + (wid & 7) * 1024;
    const char* vg = ws + WS_V + (size_t)head * 65536 + (wid & 7) * 1024;
    const unsigned ldsw = (unsigned)(uintptr_t)lds + (wid & 7) * 1024;
#define K2_CHUNK(c) do { const int cs_ = (((c) + c0) & 7) * 8192; \
        glds16(kg + cs_, lane * 16, ldsw + (c) * 8192); glds16(vg + cs_, lane * 16, ldsw + 65536 + (c) * 8192); } while (0)
    if (wid < 8) {
        asm volatile("global_load_dwordx4 %0, %1, %2" : "=v"(qf) : "v"(lane * 16), "s"(ws + WS_Q + ((size_t)head * 64 + qt) * 1024) : "memory");
        K2_CHUNK(0); K2_CHUNK(1); K2_CHUNK(2);
    }
    if (tid < 4) { *(LAS u32x4*)(lds + K2_FLAG + tid * 16) = (u32x4){0u, 0u, 0u, 0u}; }
    asm volatile("s_waitcnt lgkmcnt(0)" ::: "memory");
    BAR();

    if (wid >= 8) {
        const int tb = (bid & 1) * 16 + ((bid >> 1) & 3) * 4 + ((bid >> 3) & 3), nb = bid >> 5;
        const int ni = wid & 1, kh = (wid >> 1) & 1;
        __builtin_amdgcn_s_setprio(3);
        const unsigned vo = lane * 16;
        volatile LAS unsigned* lw = (volatile LAS unsigned*)(lds + K2_FLAG);
        {
        const char* kg2 = ws + WS_K + (size_t)head * 65536 + (wid & 3) * 2048;
        const char* vg2 = ws + WS_V + (size_t)head * 65536 + (wid & 3) * 2048;
        const unsigned l2 = (unsigned)(uintptr_t)lds + (wid & 3) * 2048;
#pragma unroll
        for (int c = 3; c < 8; ++c) {
            const int cs = ((c + c0) & 7) * 8192;
            glds16(kg2 + cs, lane * 16, l2 + c * 8192); glds16(kg2 + cs + 1024, lane * 16, l2 + c * 8192 + 1024);
            glds16(vg2 + cs, lane * 16, l2 + 65536 + c * 8192); glds16(vg2 + cs + 1024, lane * 16, l2 + 65536 + c * 8192 + 1024);
        }
    }
        WAIT_V(0);
        if (lane == 0) __hip_atomic_fetch_add((LAS unsigned*)(lw + 10), 2u, __ATOMIC_RELAXED, __HIP_MEMORY_SCOPE_WORKGROUP);
#define M_LOAD(D, sb) asm volatile("global_load_dwordx4 %0, %1, %2" : "=v"(D) : "v"(vo), "s"(sb) : "memory")
#define M_LOAD_SC1(D, sb) asm volatile("global_load_dwordx4 %0, %1, %2 sc1" : "=v"(D) : "v"(vo), "s"(sb) : "memory")
#define M_GAP() asm volatile("s_nop 7\n\ts_nop 3")
#define M_MFMA0(C, A, B) asm volatile("v_mfma_f32_32x32x16_bf16 %0, %1, %2, 0" : "=&v"(C) : "v"(A), "v"(B))
        const char* wf = ws + WS_W2 + ((size_t)(nb * 2 + ni) * NKS_F + kh * 43) * 1024;
        const char* af = ws + WS_ACT + ((size_t)(tb * 2) * NKS_F + kh * 43) * 1024;
        const int k2o = (nb * 5 + (tb & 3) * 11) % 43;
        bf16x8 fw[K2_PF], fa0[K2_PF], fa1[K2_PF];
        {
            const int wv = nb * 4 + (wid & 3), j = wv & 1, slot = wv >> 1;
            const int ft0 = slot < 11 ? 3 * slot : 33 + 2 * (slot - 11);
            const int ft2 = slot < 11 ? ft0 + 2 : ft0 + 1;
            const char* xb = ws + WS_XF + (size_t)(tb * 2 + j) * (NKS_D * 1024);
            const char* wb0 = ws + WS_W1 + (size_t)ft0 * (NKS_D * 1024);
            const char* wb2 = ws + WS_W1 + (size_t)ft2 * (NKS_D * 1024);
            const int k1o = ((tb & 3) * 2 + j) * 4;
            bf16x8 fx[6], f0[6], f1[6], f2[6];
            int kq1 = k1o;
#define P1_ISSUE(i) do { const int kk_ = kq1 * 1024; kq1 = (kq1 + 1) & 31; \
                M_LOAD(fx[i], xb + kk_); M_LOAD(f0[i], wb0 + kk_); M_LOAD(f1[i], wb0 + NKS_D * 1024 + kk_); M_LOAD(f2[i], wb2 + kk_); } while (0)
#define P1_STEP(i, n) do { asm volatile("s_waitcnt vmcnt(" #n ")" : "+v"(fx[i]), "+v"(f0[i]), "+v"(f1[i]), "+v"(f2[i]) :: "memory"); \
                A_MFMA_ACC(c[0], f0[i], fx[i]); M_GAP(); A_MFMA_ACC(c[1], f1[i], fx[i]); M_GAP(); A_MFMA_ACC(c[2], f2[i], fx[i]); } while (0)
            P1_ISSUE(0); P1_ISSUE(1); P1_ISSUE(2); P1_ISSUE(3); P1_ISSUE(4); P1_ISSUE(5);
            f32x16 c[3];
#pragma unroll
            for (int t = 0; t < 3; ++t) {
                const unsigned* bs = (const unsigned*)(b1 + 32 * (t < 2 ? ft0 + t : ft2));
#pragma unroll
                for (int e = 0; e < 8; ++e) {
                    const int d0 = (e & 1) + 4 * (e >> 1);
                    const unsigned bw = h ? bs[d0 + 2] : bs[d0];
                    c[t][2 * e] = __uint_as_float(bw << 16); c[t][2 * e + 1] = __uint_as_float(bw & 0xffff0000u);
                }
            }
            asm volatile("s_nop 1" : "+v"(c[0]), "+v"(c[1]), "+v"(c[2]));
            for (int rd = 0; rd < 4; ++rd) {
                P1_STEP(0, 20); P1_ISSUE(0); P1_STEP(1, 20); P1_ISSUE(1); P1_STEP(2, 20); P1_ISSUE(2);
                P1_STEP(3, 20); P1_ISSUE(3); P1_STEP(4, 20); P1_ISSUE(4); P1_STEP(5, 20); P1_ISSUE(5);
            }
            P1_STEP(0, 20); P1_ISSUE(0); P1_STEP(1, 20); P1_ISSUE(1);
            P1_STEP(2, 20); P1_STEP(3, 16); P1_STEP(4, 12); P1_STEP(5, 8); P1_STEP(0, 4); P1_STEP(1, 0);
#undef P1_ISSUE
#undef P1_STEP
            {
                int kk = k2o;
#pragma unroll
                for (int k = 0; k < K2_PF; ++k) { M_LOAD(fw[k], wf + kk * 1024); kk = kk == 42 ? 0 : kk + 1; }
            }
            u32x4 gx0, gx1, gg;
            M_LOAD(gx0, x + (size_t)(tb * 64 + wv * 2) * D_); M_LOAD(gx1, x + (size_t)(tb * 64 + wv * 2 + 1) * D_); M_LOAD(gg, gw);
            asm volatile("s_nop 7\n\ts_nop 7\n\ts_nop 3" : "+v"(c[0]), "+v"(c[1]), "+v"(c[2]));
            STAMPL(4);
#pragma unroll
            for (int t = 0; t < 3; ++t) {
                const int ft = t < 2 ? ft0 + t : ft2;
                f32x16 a = c[t];
#pragma unroll
                for (int e = 0; e < 16; ++e) a[e] = mish_f(a[e]);
                const char* dst = ws + WS_ACT + ((size_t)(tb * 2 + j) * NKS_F + 2 * ft) * 1024;
                const u32x4 p0 = pack8(a, 0), p1 = pack8(a, 1);
                asm volatile("global_store_dwordx4 %0, %1, %2 sc1\n\ts_nop 1" :: "v"(vo), "v"(p0), "s"(dst) : "memory");
                asm volatile("global_store_dwordx4 %0, %1, %2 offset:1024 sc1\n\ts_nop 1" :: "v"(vo), "v"(p1), "s"(dst) : "memory");
            }
            {
                asm volatile("s_waitcnt vmcnt(6)" : "+v"(gx0), "+v"(gx1), "+v"(gg) :: "memory");
                float s0_ = 0.f, s1_ = 0.f;
#pragma unroll
                for (int e = 0; e < 4; ++e) {
                    const float gl = __uint_as_float(gg[e] << 16), gh = __uint_as_float(gg[e] & 0xffff0000u);
                    s0_ += __uint_as_float(gx0[e] << 16) * gl + __uint_as_float(gx0[e] & 0xffff0000u) * gh;
                    s1_ += __uint_as_float(gx1[e] << 16) * gl + __uint_as_float(gx1[e] & 0xffff0000u) * gh;
                }
                float sv = h ? s1_ : s0_;
                sv += __shfl_xor(h ? s0_ : s1_, 32);
#pragma unroll
                for (int o = 16; o > 0; o >>= 1) sv += __shfl_xor(sv, o);
                if (r == 0) __hip_atomic_store((GAS float*)(ws + WS_G) + tb * 64 + wv * 2 + h, 1.f / (1.f + __expf(-sv)), __ATOMIC_RELAXED, __HIP_MEMORY_SCOPE_AGENT);
            }
            asm volatile("s_waitcnt vmcnt(0)" : "+v"(fw[0]), "+v"(fw[1]), "+v"(fw[2]), "+v"(fw[3]), "+v"(fw[4]), "+v"(fw[5]), "+v"(fw[6]), "+v"(fw[7]) :: "memory");
            STAMPL(5);
            unsigned old = 0;
            if (lane == 0) old = __hip_atomic_fetch_add((LAS unsigned*)(lw + 4), 1u, __ATOMIC_RELAXED, __HIP_MEMORY_SCOPE_WORKGROUP);
            const bool last = __builtin_amdgcn_readfirstlane(old) == 3u;
            GAS unsigned* cnt = (GAS unsigned*)(ws + WS_CNT) + tb * 64;
            if (last && lane == 0) __hip_atomic_fetch_add(cnt, 1u, __ATOMIC_RELAXED, __HIP_MEMORY_SCOPE_AGENT);
            if (last) {
                for (unsigned spins = 0; spins < (1u << 22); ++spins) {
                    if ((unsigned)__builtin_amdgcn_readfirstlane(__hip_atomic_load(cnt, __ATOMIC_RELAXED, __HIP_MEMORY_SCOPE_AGENT)) >= 8u) break;
                    __builtin_amdgcn_s_sleep(4);
                }
                asm volatile("s_waitcnt vmcnt(0)" ::: "memory");
                if (lane == 0) lw[5] = 1u;
            } else {
                while (lw[5] == 0u) __builtin_amdgcn_s_sleep(2);
            }
            asm volatile("s_waitcnt vmcnt(0) lgkmcnt(0)" ::: "memory");
            STAMPL(6);
        }
        STAMPL(1);
        const int w4 = wid & 3;
        bool staged = false;
#define F_STAGE() do { const char* wsrc = (const char*)(wo + (size_t)(128 * w4) * D_ + nb * 64); \
            const unsigned wvo = ((lane >> 2) * D_ + (lane & 3) * 8) * 2; \
            const unsigned wdst = (unsigned)(uintptr_t)lds + w4 * 8192; \
            _Pragma("unroll") for (int k = 0; k < 8; ++k) { \
                glds16(wsrc + k * (16 * D_ * 2), wvo, wdst + k * 1024); \
                glds16(wsrc + k * (16 * D_ * 2) + 64, wvo, wdst + 65536 + k * 1024); } \
            staged = true; } while (0)
        f32x16 c0, c1;
        int kq2 = k2o;
#pragma unroll
        for (int k = 0; k < K2_PF; ++k) { M_LOAD_SC1(fa0[k], af + kq2 * 1024); M_LOAD_SC1(fa1[k], af + (NKS_F + kq2) * 1024); kq2 = kq2 == 42 ? 0 : kq2 + 1; }
#define P2_ISSUE(i) do { const int kk_ = kq2 * 1024; kq2 = kq2 == 42 ? 0 : kq2 + 1; \
            M_LOAD(fw[i], wf + kk_); M_LOAD_SC1(fa0[i], af + kk_); M_LOAD_SC1(fa1[i], af + NKS_F * 1024 + kk_); } while (0)
#define P2_WAIT(i, n) asm volatile("s_waitcnt vmcnt(" #n ")" : "+v"(fw[i]), "+v"(fa0[i]), "+v"(fa1[i]) :: "memory")
#define P2_STEP(i, n) do { P2_WAIT(i, n); A_MFMA_ACC(c0, fw[i], fa0[i]); M_GAP(); A_MFMA_ACC(c1, fw[i], fa1[i]); } while (0)
        P2_WAIT(0, 14); M_MFMA0(c0, fw[0], fa0[0]); M_MFMA0(c1, fw[0], fa1[0]); P2_ISSUE(0);
        P2_STEP(1, 15); P2_ISSUE(1); P2_STEP(2, 16); P2_ISSUE(2); P2_STEP(3, 17); P2_ISSUE(3);
        P2_STEP(4, 18); P2_ISSUE(4); P2_STEP(5, 19); P2_ISSUE(5); P2_STEP(6, 20); P2_ISSUE(6); P2_STEP(7, 21); P2_ISSUE(7);
        for (int rd = 0; rd < 3; ++rd) {
            if (!staged && (unsigned)__builtin_amdgcn_readfirstlane(lw[11]) >= 8u) F_STAGE();
            P2_STEP(0, 21); P2_ISSUE(0); P2_STEP(1, 21); P2_ISSUE(1); P2_STEP(2, 21); P2_ISSUE(2); P2_STEP(3, 21); P2_ISSUE(3);
            P2_STEP(4, 21); P2_ISSUE(4); P2_STEP(5, 21); P2_ISSUE(5); P2_STEP(6, 21); P2_ISSUE(6); P2_STEP(7, 21); P2_ISSUE(7);
        }
        P2_STEP(0, 21); P2_ISSUE(0); P2_STEP(1, 21); P2_ISSUE(1); P2_STEP(2, 21); P2_ISSUE(2);
        P2_STEP(3, 21); P2_STEP(4, 18); P2_STEP(5, 15); P2_STEP(6, 12); P2_STEP(7, 9); P2_STEP(0, 6); P2_STEP(1, 3); P2_STEP(2, 0);
#undef P2_ISSUE
#undef P2_WAIT
#undef P2_STEP
        asm volatile("s_nop 7\n\ts_nop 7\n\ts_nop 3" : "+v"(c0), "+v"(c1));
        LAS f32x4* red = (LAS f32x4*)(lds + K2_RED + ni * 8192);
        if (kh == 1) {
#pragma unroll
            for (int qd = 0; qd < 4; ++qd) {
                red[qd * 64 + lane] = (f32x4){c0[4 * qd], c0[4 * qd + 1], c0[4 * qd + 2], c0[4 * qd + 3]};
                red[(4 + qd) * 64 + lane] = (f32x4){c1[4 * qd], c1[4 * qd + 1], c1[4 * qd + 2], c1[4 * qd + 3]};
            }
            asm volatile("s_waitcnt lgkmcnt(0)" ::: "memory");
            if (lane == 0) lw[ni] = 1u;
        } else {
            while (lw[ni] == 0u) __builtin_amdgcn_s_sleep(2);
            asm volatile("" ::: "memory");
#pragma unroll
            for (int qd = 0; qd < 4; ++qd) {
                red[qd * 64 + lane] += (f32x4){c0[4 * qd], c0[4 * qd + 1], c0[4 * qd + 2], c0[4 * qd + 3]};
                red[(4 + qd) * 64 + lane] += (f32x4){c1[4 * qd], c1[4 * qd + 1], c1[4 * qd + 2], c1[4 * qd + 3]};
            }
            asm volatile("s_waitcnt lgkmcnt(0)" ::: "memory");
            if (lane == 0) __hip_atomic_fetch_add((LAS unsigned*)(lw + 2), 1u, __ATOMIC_RELAXED, __HIP_MEMORY_SCOPE_WORKGROUP);
        }
        STAMPL(7);
        if (!staged) {
            while (lw[11] < 8u) __builtin_amdgcn_s_sleep(4);
            asm volatile("" ::: "memory");
            F_STAGE();
        }
#undef F_STAGE
        float g0, g1;
        asm volatile("global_load_dword %0, %2, %3 sc1\n\tglobal_load_dword %1, %2, %3 offset:128 sc1"
                     : "=&v"(g0), "=&v"(g1) : "v"(r * 4), "s"(ws + WS_G + tb * 256) : "memory");
        {
            GAS unsigned* oc = (GAS unsigned*)(ws + WS_CNT) + (32 + (tb >> 2) * 4 + w4) * 64;
            for (unsigned spins = 0; spins < (1u << 22); ++spins) {
                if ((unsigned)__builtin_amdgcn_readfirstlane(__hip_atomic_load(oc, __ATOMIC_RELAXED, __HIP_MEMORY_SCOPE_AGENT)) >= 8u) break;
                __builtin_amdgcn_s_sleep(2);
            }
        }
        asm volatile("s_waitcnt vmcnt(0) lgkmcnt(0)" : "+v"(g0), "+v"(g1) :: "memory");
        bf16x8 fo0[8], fo1[8];
        {
            const char* ob = ws + WS_O + ((size_t)(tb * 2) * NKS_D + w4 * 8) * 1024;
#pragma unroll
            for (int k = 0; k < 8; ++k) { M_LOAD_SC1(fo0[k], ob + k * 1024); M_LOAD_SC1(fo1[k], ob + (NKS_D + k) * 1024); }
        }
        f32x16 fa[4];
        {
            const LAS char* rb = lds + w4 * 8192 + (4 * h + ((lane & 15) >> 2)) * 64 + ((lane >> 4) & 1) * 32 + (lane & 3) * 8;
#define F_STEP(k, n) do { \
                v4i16_t l0 = __builtin_amdgcn_ds_read_tr16_b64_v4i16((LAS v4i16_t*)(rb + (k) * 1024)); \
                v4i16_t h0 = __builtin_amdgcn_ds_read_tr16_b64_v4i16((LAS v4i16_t*)(rb + (k) * 1024 + 512)); \
                v4i16_t l1 = __builtin_amdgcn_ds_read_tr16_b64_v4i16((LAS v4i16_t*)(rb + 65536 + (k) * 1024)); \
                v4i16_t h1 = __builtin_amdgcn_ds_read_tr16_b64_v4i16((LAS v4i16_t*)(rb + 65536 + (k) * 1024 + 512)); \
                const bf16x8 w0_ = __builtin_bit_cast(bf16x8, __builtin_shufflevector(l0, h0, 0, 1, 2, 3, 4, 5, 6, 7)); \
                const bf16x8 w1_ = __builtin_bit_cast(bf16x8, __builtin_shufflevector(l1, h1, 0, 1, 2, 3, 4, 5, 6, 7)); \
                asm volatile("s_waitcnt vmcnt(" #n ")" : "+v"(fo0[k]), "+v"(fo1[k]) :: "memory"); \
                if ((k) == 0) { M_MFMA0(fa[0], w0_, fo0[k]); M_MFMA0(fa[1], w0_, fo1[k]); M_MFMA0(fa[2], w1_, fo0[k]); M_MFMA0(fa[3], w1_, fo1[k]); } \
                else { A_MFMA_ACC(fa[0], w0_, fo0[k]); A_MFMA_ACC(fa[1], w0_, fo1[k]); A_MFMA_ACC(fa[2], w1_, fo0[k]); A_MFMA_ACC(fa[3], w1_, fo1[k]); } \
            } while (0)
            F_STEP(0, 14); F_STEP(1, 12); F_STEP(2, 10); F_STEP(3, 8); F_STEP(4, 6); F_STEP(5, 4); F_STEP(6, 2); F_STEP(7, 0);
#undef F_STEP
        }
        asm volatile("s_nop 7\n\ts_nop 7\n\ts_nop 3" : "+v"(fa[0]), "+v"(fa[1]), "+v"(fa[2]), "+v"(fa[3]));
        while (lw[7] < 8u) __builtin_amdgcn_s_sleep(2);
        asm volatile("" ::: "memory");
        LAS f32x4* fr = (LAS f32x4*)(lds + 32768);
#define F_SLOT(w, t, qd) ((((w) & 1) * 16 + (t) * 4 + (qd)) * 64 + ((w) >> 1) * 4096 + lane)
#pragma unroll
        for (int t = 0; t < 4; ++t)
#pragma unroll
            for (int qd = 0; qd < 4; ++qd)
                fr[F_SLOT(w4, t, qd)] = (f32x4){fa[t][4 * qd], fa[t][4 * qd + 1], fa[t][4 * qd + 2], fa[t][4 * qd + 3]};
        asm volatile("s_waitcnt lgkmcnt(0)" ::: "memory");
        if (lane == 0) __hip_atomic_fetch_add((LAS unsigned*)(lw + 3), 1u, __ATOMIC_RELAXED, __HIP_MEMORY_SCOPE_WORKGROUP);
        while (lw[3] < 4u || lw[2] < 2u) __builtin_amdgcn_s_sleep(1);
        asm volatile("" ::: "memory");
        {
            const float g = (w4 & 1) ? g1 : g0;
            const LAS f32x4* bk = (const LAS f32x4*)(lds + K2_RED) + (size_t)w4 * 256 + lane;
            u16* op = out + (size_t)(tb * 64 + (w4 & 1) * 32 + r) * D_ + nb * 64 + (w4 >> 1) * 32 + 4 * h;
#pragma unroll
            for (int qd = 0; qd < 4; ++qd) {
                f32x4 sa = fr[F_SLOT(0, w4, qd)] + fr[F_SLOT(1, w4, qd)] + fr[F_SLOT(2, w4, qd)] + fr[F_SLOT(3, w4, qd)];
                sa = sa * g + bk[qd * 64] * (1.f - g);
                *(u32x2*)(op + 8 * qd) = (u32x2){pk2(sa[0], sa[1]), pk2(sa[2], sa[3])};
            }
        }
#undef F_SLOT
        STAMPL(3);
        STAMPL_FLUSH(ws, 1);
        return;
    }

    if (tid < 40) {
        const unsigned c = (tid & 1) ? 0u : 0x3f803f80u;
        *(LAS u32x4*)(lds + K2_CONST + (tid >> 1) * 512 + (tid & 1) * 16) = (u32x4){c, c, c, c};
    }
    const bool vact = r < 16;
    const LAS char* kp = lds + lane * 16;
    const LAS char* vp = vact ? lds + 65536 + h * 256 + r * 16 : lds + K2_CONST + (r == 16 ? 0 : 16);
    const int vinc = vact ? 8192 : 0;

    f32x16 ot = (f32x16){};
    f32x16 negm, sa, sb;
    bf16x8 ka, kb, va0, va1, vb0, vb1;
    if (X_NOLDS) { vb0 = *(const LAS bf16x8*)(vp); vb1 = *(const LAS bf16x8*)(vp + 512); }
    float m;
    volatile LAS unsigned* cw = (volatile LAS unsigned*)(lds + K2_FLAG) + 8;
#define K2_SIGNAL(c) do { if (lane == 0) __hip_atomic_fetch_add((LAS unsigned*)(cw + (c)), 1u, __ATOMIC_RELAXED, __HIP_MEMORY_SCOPE_WORKGROUP); } while (0)
#define K2_NEED(c) do { while (cw[c] < 8u) __builtin_amdgcn_s_sleep(1); asm volatile("" ::: "memory"); } while (0)
    asm volatile("s_waitcnt vmcnt(0) lgkmcnt(0)" : "+v"(qf) :: "memory");
    K2_SIGNAL(1);
    K2_NEED(1);
    STAMPL(1);
    {
        ka = *(const LAS bf16x8*)kp;
        kb = *(const LAS bf16x8*)(kp + 1024);
        va0 = *(const LAS bf16x8*)(vp); va1 = *(const LAS bf16x8*)(vp + 512);
        f32x16 s = MFMA(ka, qf, (f32x16){});
        float mx = fmaxf(s[0], s[1]);
#pragma unroll
        for (int e = 2; e < 16; ++e) mx = fmaxf(mx, s[e]);
        mx = fmaxf(mx, __shfl_xor(mx, 32));
        m = mx;
#pragma unroll
        for (int e = 0; e < 16; ++e) negm[e] = -m;
        asm volatile("s_nop 3" : "+v"(negm));
        A_MFMA_S(sa, ka);
        asm volatile("s_nop 7\n\ts_nop 7" : "+v"(sa));
    }
    const LAS char* kq = kp;
    const LAS char* vq = vp;
    int g = 0;
    for (; g < 8; ++g) {
        if (g == 2) K2_NEED(2);
        else if (g == 4) K2_SIGNAL(3);
        if (g == 0) { if (wid >= 4) __builtin_amdgcn_s_setprio(2); else __builtin_amdgcn_s_setprio(1); }
        else if (g == 5 && wid >= 4) __builtin_amdgcn_s_setprio(0);
        const LAS char* kq2 = (g == 7) ? kp - 8192 : kq;
        const LAS char* vq2 = (g == 7) ? vp - vinc : vq;
        asm volatile("" : "+v"(kq), "+v"(vq), "+v"(kq2), "+v"(vq2));
        f32x16 og;
        float e[16];
        u32x4 p0, p1;
#define K2_TILE(SC, SN, KC, KN, VC0, VP1, VN0, VN1, t) do { \
            if ((t) > 0) A_MFMA_PV(VP1, p1); \
            if (!X_NOLDS) { \
            KN = ((t) + 2 < 8) ? *(const LAS bf16x8*)(kq + ((t) + 2) * 1024) : *(const LAS bf16x8*)(kq2 + ((t) + 2) * 1024); \
            if ((t) + 1 < 8) { VN0 = *(const LAS bf16x8*)(vq + ((t) + 1) * 1024); VN1 = *(const LAS bf16x8*)(vq + ((t) + 1) * 1024 + 512); } \
            else { VN0 = *(const LAS bf16x8*)(vq2 + ((t) + 1) * 1024); VN1 = *(const LAS bf16x8*)(vq2 + ((t) + 1) * 1024 + 512); } } \
            A_EXP4(e, SC, 0); A_EXP4(e, SC, 4); \
            A_MFMA_S(SN, KC); \
            A_CVT4(p0, e, 0); \
            A_EXP4(e, SC, 8); \
            if ((t) == 0) A_MFMA_PV0(VC0, p0); else A_MFMA_PV(VC0, p0); \
            A_EXP4(e, SC, 12); \
            A_CVT4(p1, e, 8); \
            asm volatile("s_nop 1"); \
        } while (0)
#pragma unroll
        for (int t = 0; t < 8; t += 2) {
            K2_TILE(sa, sb, kb, ka, va0, vb1, vb0, vb1, t);
            K2_TILE(sb, sa, ka, kb, vb0, va1, va0, va1, t + 1);
        }
        A_MFMA_PV(vb1, p1);
        asm volatile("s_nop 7\n\ts_nop 7\n\ts_nop 3" : "+v"(og));
#undef K2_TILE
        if (__builtin_expect(__any(!(og[8] <= K2_LIM)), 0)) break;
#pragma unroll
        for (int i = 0; i < 9; ++i) ot[i] += og[i];
        kq += 8192; vq += vinc;
    }
    if (__builtin_expect(g < 8, 0)) {
        K2_NEED(2);
        for (int t = g * 8; t < 64; ++t) {
            bf16x8 kf = *(const LAS bf16x8*)(kp + t * 1024);
            const LAS char* vt = vp + (vact ? t * 1024 : 0);
            vb0 = *(const LAS bf16x8*)(vt); vb1 = *(const LAS bf16x8*)(vt + 512);
            f32x16 s = MFMA(kf, qf, (f32x16){});
            float mx = fmaxf(s[0], s[1]);
#pragma unroll
            for (int i = 2; i < 16; ++i) mx = fmaxf(mx, s[i]);
            mx = fmaxf(mx, __shfl_xor(mx, 32));
            float mn = fmaxf(m, mx);
            float al = __builtin_amdgcn_exp2f(m - mn);
            m = mn;
            ot = ot * al;
#pragma unroll
            for (int i = 0; i < 16; ++i) s[i] = __builtin_amdgcn_exp2f(s[i] - m);
            ot = MFMA(vb0, __builtin_bit_cast(bf16x8, pack8(s, 0)), ot);
            ot = MFMA(vb1, __builtin_bit_cast(bf16x8, pack8(s, 1)), ot);
        }
        asm volatile("s_waitcnt lgkmcnt(0)" ::: "memory");
        if (g < 4) K2_SIGNAL(3);
    }
    STAMPL(2);
    float l = __shfl(ot[8], r);
    float rl = 1.f / l;
    f32x16 on = ot * rl;
    {
        const u32x4 po = pack8(on, 0);
        asm volatile("global_store_dwordx4 %0, %1, %2 sc1\n\ts_nop 1\n\ts_waitcnt vmcnt(0)"
                     :: "v"(lane * 16), "v"(po), "s"(ws + WS_O + ((size_t)qt * NKS_D + head) * 1024) : "memory");
        unsigned old = 0;
        if (lane == 0) old = __hip_atomic_fetch_add((LAS unsigned*)(cw - 1), 1u, __ATOMIC_RELAXED, __HIP_MEMORY_SCOPE_WORKGROUP);
        if (__builtin_amdgcn_readfirstlane(old) == 7u && lane == 0)
            __hip_atomic_fetch_add((GAS unsigned*)(ws + WS_CNT) + (32 + qb * 4 + (head >> 3)) * 64, 1u, __ATOMIC_RELAXED, __HIP_MEMORY_SCOPE_AGENT);
    }
    STAMPL(3);
    STAMPL_FLUSH(ws, 1);
}

extern "C" void kernel_launch(void* const* d_in, const int* in_sizes, int n_in,
                              void* d_out, int out_size, void* d_ws, size_t ws_size,
                              hipStream_t stream) {
    const u16* x = (const u16*)d_in[0];
    const u16* wqkv = (const u16*)d_in[1];
    const u16* wo = (const u16*)d_in[2];
    const u16* w1 = (const u16*)d_in[4];
    const u16* b1 = (const u16*)d_in[5];
    const u16* w2 = (const u16*)d_in[6];
    const u16* gw = (const u16*)d_in[7];
    char* ws = (char*)d_ws;
    (void)hipFuncSetAttribute((const void*)k1_kernel, hipFuncAttributeMaxDynamicSharedMemorySize, K1_LDS);
    (void)hipFuncSetAttribute((const void*)k2_kernel, hipFuncAttributeMaxDynamicSharedMemorySize, K2_LDS);
    k1_kernel<<<256, 1024, K1_LDS, stream>>>(x, wqkv, w1, w2, ws);
    k2_kernel<<<256, 768, K2_LDS, stream>>>(ws, b1, wo, x, gw, (u16*)d_out);
}
```

```cpp
#include <hip/hip_runtime.h>
#include <hip/hip_bf16.h>
#include <stdint.h>

typedef __attribute__((ext_vector_type(8))) __bf16 bf16x8;
typedef __attribute__((ext_vector_type(2))) __bf16 bf16x2;
typedef __attribute__((ext_vector_type(2))) float f32x2;
typedef __attribute__((ext_vector_type(4))) float f32x4;
typedef __attribute__((ext_vector_type(16))) float f32x16;
typedef __attribute__((ext_vector_type(4))) unsigned u32x4;
typedef __attribute__((ext_vector_type(2))) unsigned u32x2;
typedef short v4i16_t __attribute__((ext_vector_type(4)));
typedef short v8i16_t __attribute__((ext_vector_type(8)));
typedef unsigned short u16;
#define LAS __attribute__((address_space(3)))
#define GAS __attribute__((address_space(1)))
#define DI __device__ __forceinline__
#define MFMA(a, b, c) __builtin_amdgcn_mfma_f32_32x32x16_bf16((a), (b), (c), 0, 0, 0)

constexpr int D_ = 512, F_ = 1376;
constexpr int NKS_F = F_ / 16;
constexpr int NKS_D = D_ / 16;

constexpr size_t WS_Q = 0;
constexpr size_t WS_K = WS_Q + (size_t)2 * 1024 * 1024;
constexpr size_t WS_V = WS_K + (size_t)2 * 1024 * 1024;
constexpr size_t WS_O = WS_V + (size_t)2 * 1024 * 1024;
constexpr size_t WS_ACT = WS_O + (size_t)2 * 1024 * 1024;
constexpr size_t WS_W2 = WS_ACT + (size_t)64 * NKS_F * 1024;
constexpr size_t WS_W1 = WS_W2 + (size_t)16 * NKS_F * 1024;
constexpr size_t WS_XF = WS_W1 + (size_t)43 * NKS_D * 1024;
constexpr size_t WS_CNT = WS_XF + (size_t)64 * NKS_D * 1024;
constexpr size_t WS_G = WS_CNT + 64 * 256;

DI unsigned pk2(float a, float b) { bf16x2 r = __builtin_convertvector((f32x2){a, b}, bf16x2); return __builtin_bit_cast(unsigned, r); }
DI u32x4 pack8(const f32x16& x, int s) {
    u32x4 p;
    p[0] = pk2(x[8 * s + 0], x[8 * s + 1]); p[1] = pk2(x[8 * s + 2], x[8 * s + 3]);
    p[2] = pk2(x[8 * s + 4], x[8 * s + 5]); p[3] = pk2(x[8 * s + 6], x[8 * s + 7]);
    return p;
}
DI void glds16(const char* sbase, unsigned voff, unsigned lds_base) {
    unsigned sv;
    asm volatile("s_mov_b32 %0, m0\n\ts_mov_b32 m0, %3\n\ts_nop 0\n\tglobal_load_lds_dwordx4 %1, %2\n\ts_mov_b32 m0, %0"
                 : "=&s"(sv) : "v"(voff), "s"(sbase), "s"(lds_base) : "memory");
}
#define WAIT_V(n) asm volatile("s_waitcnt vmcnt(" #n ")" ::: "memory")
#define BAR() __builtin_amdgcn_s_barrier()

DI float mish_f(float h) {
    const float n = __builtin_amdgcn_exp2f(fminf(h, 20.f) * 1.4426950408889634f);
    const float w = n * (n + 2.f);
    return h * w * __builtin_amdgcn_rcpf(w + 2.f);
}

constexpr int K1_SA = 64 * 64 * 2;
constexpr int K1_SB = 3 * 64 * 64 * 2;
constexpr int K1_BOFF = 8 * K1_SA;
constexpr int K1_LDS = K1_BOFF + 4 * K1_SB;
constexpr int NFRAG = 16 * NKS_F + 43 * NKS_D;

#define A_MFMA_ACC(C, A, B) asm volatile("v_mfma_f32_32x32x16_bf16 %0, %1, %2, %0" : "+v"(C) : "v"(A), "v"(B))

struct K1Frag { bf16x8 a[4], b[4]; };
DI void k1_ldfrag(K1Frag& f, const LAS char* lds, int it, const int (&aoff)[4], int boff) {
    int ao_ = it * K1_SA, bo_ = (it & 3) * K1_SB;
    asm volatile("" : "+s"(ao_), "+s"(bo_));
#pragma unroll
    for (int ks = 0; ks < 4; ++ks) {
        f.a[ks] = *(const LAS bf16x8*)(lds + ao_ + aoff[ks]);
        v4i16_t lo = __builtin_amdgcn_ds_read_tr16_b64_v4i16((LAS v4i16_t*)(lds + bo_ + boff + ks * 2048));
        v4i16_t hi = __builtin_amdgcn_ds_read_tr16_b64_v4i16((LAS v4i16_t*)(lds + bo_ + boff + ks * 2048 + 512));
        v8i16_t w = __builtin_shufflevector(lo, hi, 0, 1, 2, 3, 4, 5, 6, 7);
        f.b[ks] = __builtin_bit_cast(bf16x8, w);
    }
}

__global__ __launch_bounds__(1024) void k1_kernel(const u16* __restrict__ x, const u16* __restrict__ wqkv, const u16* __restrict__ w1,
                                                  const u16* __restrict__ w2, char* __restrict__ ws) {
    extern __shared__ __attribute__((aligned(16))) char smem[];
    LAS char* lds = (LAS char*)smem;
    const int tid = threadIdx.x, lane = tid & 63;
    const int wid = __builtin_amdgcn_readfirstlane(tid >> 6);
    const int bid = blockIdx.x;
    const int xj = (bid & 6) + (bid >> 7), mt = (bid & 1) * 16 + ((bid >> 3) & 15);
    const int r = lane & 31, h = lane >> 5;
    const int s0 = mt & 7;
    const unsigned lds0 = (unsigned)(uintptr_t)lds;

    if (wid >= 12) {
        const int lw = wid - 12;
        const int row = lw * 16 + (lane >> 3);
        const char* abase = (const char*)(x + (size_t)(mt * 64) * D_);
        const char* bbase = (const char*)(wqkv + 64 * xj);
        const unsigned ao0 = (row * D_ + (((lane & 7) ^ ((row >> 1) & 7)) << 3)) * 2;
        const unsigned ao1 = ((row + 8) * D_ + (((lane & 7) ^ (((row + 8) >> 1) & 7)) << 3)) * 2;
        const unsigned bo0 = (row * (3 * D_) + (((lane & 7) ^ (((row >> 1) & 1) << 2)) << 3)) * 2;
        const unsigned bo1 = bo0 + 8 * (3 * D_) * 2;
        const unsigned adst = lds0 + lw * 2048, bdst = lds0 + K1_BOFF + lw * 2048;
#define K1_ISSUE_A(it) do { const int st_ = ((it) + s0) & 7; \
            glds16(abase + st_ * 128, ao0, adst + (it) * K1_SA); glds16(abase + st_ * 128, ao1, adst + (it) * K1_SA + 1024); } while (0)
#define K1_ISSUE_B(it) do { const int st_ = ((it) + s0) & 7; const int bf_ = ((it) & 3) * K1_SB; \
            const char* b_ = bbase + (size_t)st_ * (128 * 3 * D_); \
            _Pragma("unroll") for (int j_ = 0; j_ < 3; ++j_) { \
                glds16(b_ + j_ * (2 * D_), bo0, bdst + bf_ + j_ * 8192); glds16(b_ + j_ * (2 * D_), bo1, bdst + bf_ + j_ * 8192 + 1024); } } while (0)
        K1_ISSUE_A(0); K1_ISSUE_B(0); K1_ISSUE_A(1); K1_ISSUE_B(1);
        WAIT_V(8);  BAR(); K1_ISSUE_A(2); K1_ISSUE_B(2); K1_ISSUE_A(3); K1_ISSUE_B(3);
        WAIT_V(16); BAR(); K1_ISSUE_B(4); K1_ISSUE_A(4); K1_ISSUE_A(5);
        WAIT_V(18); BAR(); K1_ISSUE_B(5); K1_ISSUE_A(6); K1_ISSUE_A(7);
        WAIT_V(20); BAR(); K1_ISSUE_B(6);
        WAIT_V(18); BAR(); K1_ISSUE_B(7);
        WAIT_V(16); BAR();
        WAIT_V(6);  BAR();
        WAIT_V(0);  BAR();
#undef K1_ISSUE_A
#undef K1_ISSUE_B
        if (bid == 0 && lw == 0) ((unsigned*)(ws + WS_CNT))[lane * 64] = 0u;
        return;
    }

    const int mi = wid & 1, ni = wid >> 1;
    const int tt = mt * 2 + mi;
    const int rf = bid * 12 + wid;
    const bool nat = rf >= 16 * NKS_F;
#define K1_ADDR() int ln = lane; asm volatile("" : "+v"(ln)); \
    const int arow = mi * 32 + (ln & 31), asw = (arow >> 1) & 7; \
    int aoff[4]; \
    _Pragma("unroll") for (int ks = 0; ks < 4; ++ks) aoff[ks] = arow * 128 + (((2 * ks + (ln >> 5)) ^ asw) << 4); \
    const int boff = K1_BOFF + (ni >> 1) * 8192 + (8 * (ln >> 5) + ((ln & 15) >> 2)) * 128 + (((ni & 1) ^ ((ln & 15) >> 3)) << 6) + ((ln >> 4) & 1) * 32 + (ln & 3) * 8;
#define K1_RLOAD() do { int f = rf < NFRAG ? rf : 0; const u16* wsrc; int ld; \
        if (f < 16 * NKS_F) { wsrc = w2 + (size_t)(16 * (f % NKS_F)) * D_ + (f / NKS_F) * 32; ld = D_; } \
        else { f -= 16 * NKS_F; wsrc = w1 + (size_t)(16 * (f & 31)) * F_ + (f >> 5) * 32; ld = F_; } \
        glds16((const char*)wsrc, ((ln >> 2) * ld + (ln & 3) * 8) * 2, lds0 + wid * 1024); } while (0)
#define K1_REPACK() do { asm volatile("s_waitcnt vmcnt(0)" ::: "memory"); \
        const LAS char* rb = lds + wid * 1024 + ((nat ? 8 : 4) * (ln >> 5) + ((ln & 15) >> 2)) * 64 + ((ln >> 4) & 1) * 32 + (ln & 3) * 8; \
        v4i16_t lo = __builtin_amdgcn_ds_read_tr16_b64_v4i16((LAS v4i16_t*)(rb)); \
        v4i16_t hi = __builtin_amdgcn_ds_read_tr16_b64_v4i16((LAS v4i16_t*)(rb + (nat ? 256 : 512))); \
        v8i16_t w = __builtin_shufflevector(lo, hi, 0, 1, 2, 3, 4, 5, 6, 7); \
          \
        if (rf < NFRAG) asm volatile("global_store_dwordx4 %0, %1, %2 sc1\n\ts_nop 1" :: "v"(ln * 16), "v"(w), "s"(ws + WS_W2 + (size_t)rf * 1024) : "memory"); \
    } while (0)
    if (wid < 8) {
        K1_ADDR();
        f32x16 acc = (f32x16){};
        K1Frag f0, f1;
        BAR();
        k1_ldfrag(f0, lds, 0, aoff, boff);
#define K1_STAGE(FC, FN, it) do { \
            if ((it) < 7) { asm volatile("s_waitcnt lgkmcnt(0)" ::: "memory"); BAR(); k1_ldfrag(FN, lds, (it) + 1, aoff, boff); } \
            A_MFMA_ACC(acc, FC.b[0], FC.a[0]); A_MFMA_ACC(acc, FC.b[1], FC.a[1]); \
            A_MFMA_ACC(acc, FC.b[2], FC.a[2]); A_MFMA_ACC(acc, FC.b[3], FC.a[3]); \
        } while (0)
        K1_STAGE(f0, f1, 0); K1_STAGE(f1, f0, 1);
        K1_RLOAD();
        K1_STAGE(f0, f1, 2); K1_STAGE(f1, f0, 3); K1_STAGE(f0, f1, 4);
        K1_REPACK();
        K1_STAGE(f1, f0, 5); K1_STAGE(f0, f1, 6); K1_STAGE(f1, f0, 7);
#undef K1_STAGE
        asm volatile("s_nop 7\n\ts_nop 7\n\ts_nop 3" : "+v"(acc));
        const int ix = 2 * xj + (ni & 1);
        if (ni < 2) acc = acc * (0.25f * 1.4426950408889634f);
        char* dst = ws + (ni < 2 ? WS_Q : WS_K);
#pragma unroll
        for (int s = 0; s < 2; ++s)
            { const u32x4 pv = pack8(acc, s); asm volatile("global_store_dwordx4 %0, %1, off sc1\n\ts_nop 1" :: "v"(dst + ((size_t)(2 * ix + s) * 64 + tt) * 1024 + lane * 16), "v"(pv) : "memory"); }
    } else {
        char* xf = ws + WS_XF + ((size_t)tt * NKS_D + (ni & 1) * 2) * 1024 + lane * 16;
        K1_ADDR();
        f32x16 acc = (f32x16){};
        K1Frag f0, f1;
        BAR();
        k1_ldfrag(f0, lds, 0, aoff, boff);
#define K1_STAGE(FC, FN, it) do { \
            if ((it) < 7) { asm volatile("s_waitcnt lgkmcnt(0)" ::: "memory"); BAR(); k1_ldfrag(FN, lds, (it) + 1, aoff, boff); } \
            A_MFMA_ACC(acc, FC.a[0], FC.b[0]); A_MFMA_ACC(acc, FC.a[1], FC.b[1]); \
            A_MFMA_ACC(acc, FC.a[2], FC.b[2]); A_MFMA_ACC(acc, FC.a[3], FC.b[3]); \
            if ((((it) + s0) & 7) == xj) { char* xd_ = xf + (size_t)xj * 4096; \
                          if (ni & 1) asm volatile("global_store_dwordx4 %0, %1, off sc1\n\tglobal_store_dwordx4 %0, %2, off offset:1024 sc1\n\ts_nop 1" :: "v"(xd_), "v"(FC.a[2]), "v"(FC.a[3]) : "memory"); \
                          else asm volatile("global_store_dwordx4 %0, %1, off sc1\n\tglobal_store_dwordx4 %0, %2, off offset:1024 sc1\n\ts_nop 1" :: "v"(xd_), "v"(FC.a[0]), "v"(FC.a[1]) : "memory"); } \
        } while (0)
        K1_STAGE(f0, f1, 0); K1_STAGE(f1, f0, 1);
        K1_RLOAD();
        K1_STAGE(f0, f1, 2); K1_STAGE(f1, f0, 3); K1_STAGE(f0, f1, 4);
        K1_REPACK();
        K1_STAGE(f1, f0, 5); K1_STAGE(f0, f1, 6); K1_STAGE(f1, f0, 7);
#undef K1_STAGE
        asm volatile("s_nop 7\n\ts_nop 7\n\ts_nop 3" : "+v"(acc));
        const int head = 2 * (2 * xj + (ni & 1)) + (r >> 4), d = r & 15;
#pragma unroll
        for (int s = 0; s < 2; ++s)
            { const u32x4 pv = pack8(acc, s); asm volatile("global_store_dwordx4 %0, %1, off sc1\n\ts_nop 1" :: "v"(ws + WS_V + ((size_t)head * 128 + 2 * tt + s) * 512 + h * 256 + d * 16), "v"(pv) : "memory"); }
    }
#undef K1_ADDR
#undef K1_RLOAD
#undef K1_REPACK
}

constexpr int K2_CONST = 128 * 1024;
constexpr int K2_RED = K2_CONST + 20 * 512;
constexpr int K2_FLAG = K2_RED + 16384;
constexpr int K2_LDS = K2_FLAG + 64;
constexpr float K2_LIM = 65536.f;
constexpr int K2_PF = 8;

#define A_MFMA_S(D, K) asm volatile("v_mfma_f32_32x32x16_bf16 %0, %1, %2, %3" : "=&v"(D) : "v"(K), "v"(qf), "v"(negm) : "memory")
#define A_MFMA_PV(V, P) asm volatile("v_mfma_f32_32x32x16_bf16 %0, %1, %2, %0" : "+v"(og) : "v"(V), "v"(P))
#define A_MFMA_PV0(V, P) asm volatile("v_mfma_f32_32x32x16_bf16 %0, %1, %2, 0" : "=&v"(og) : "v"(V), "v"(P))
#define A_EXP4(E, S, i) asm volatile("v_exp_f32 %0, %4\n\tv_exp_f32 %1, %5\n\tv_exp_f32 %2, %6\n\tv_exp_f32 %3, %7" \
        : "=&v"(E[i]), "=&v"(E[i + 1]), "=&v"(E[i + 2]), "=&v"(E[i + 3]) : "v"(S[i]), "v"(S[i + 1]), "v"(S[i + 2]), "v"(S[i + 3]))
#define A_CVT4(P, E, i) asm volatile("v_cvt_pk_bf16_f32 %0, %4, %5\n\tv_cvt_pk_bf16_f32 %1, %6, %7\n\tv_cvt_pk_bf16_f32 %2, %8, %9\n\tv_cvt_pk_bf16_f32 %3, %10, %11" \
        : "=&v"(P[0]), "=&v"(P[1]), "=&v"(P[2]), "=&v"(P[3]) \
        : "v"(E[i]), "v"(E[i + 1]), "v"(E[i + 2]), "v"(E[i + 3]), "v"(E[i + 4]), "v"(E[i + 5]), "v"(E[i + 6]), "v"(E[i + 7]))

__global__ __launch_bounds__(768) void k2_kernel(char* __restrict__ ws, const u16* __restrict__ b1, const u16* __restrict__ wo,
                                                 const u16* __restrict__ x, const u16* __restrict__ gw, u16* __restrict__ out) {
    extern __shared__ __attribute__((aligned(16))) char smem[];
    LAS char* lds = (LAS char*)smem;
    const int tid = threadIdx.x, lane = tid & 63;
    const int wid = __builtin_amdgcn_readfirstlane(tid >> 6);
    const int bid = blockIdx.x;
    const int r = lane & 31, h = lane >> 5;

    const int head = ((bid >> 1) & 3) * 8 + ((bid >> 3) & 7), qb = (bid & 1) * 4 + (bid >> 6);
    const int qt = qb * 8 + (wid & 7);
    const int c0 = (bid & 1) * 4;
    bf16x8 qf;

    const char* kg = ws + WS_K + (size_t)head * 65536 + (wid & 7) * 1024;
    const char* vg = ws + WS_V + (size_t)head * 65536 + (wid & 7) * 1024;
    const unsigned ldsw = (unsigned)(uintptr_t)lds + (wid & 7) * 1024;
#define K2_CHUNK(c) do { const int cs_ = (((c) + c0) & 7) * 8192; \
        glds16(kg + cs_, lane * 16, ldsw + (c) * 8192); glds16(vg + cs_, lane * 16, ldsw + 65536 + (c) * 8192); } while (0)
    if (wid < 8) {
        asm volatile("global_load_dwordx4 %0, %1, %2" : "=v"(qf) : "v"(lane * 16), "s"(ws + WS_Q + ((size_t)head * 64 + qt) * 1024) : "memory");
        K2_CHUNK(0); K2_CHUNK(1); K2_CHUNK(2);
    }
    if (tid < 4) { *(LAS u32x4*)(lds + K2_FLAG + tid * 16) = (u32x4){0u, 0u, 0u, 0u}; }
    asm volatile("s_waitcnt lgkmcnt(0)" ::: "memory");
    BAR();

    if (wid >= 8) {
        const int tb = (bid & 1) * 16 + ((bid >> 1) & 3) * 4 + ((bid >> 3) & 3), nb = bid >> 5;
        const int ni = wid & 1, kh = (wid >> 1) & 1;
        __builtin_amdgcn_s_setprio(3);
        const unsigned vo = lane * 16;
        volatile LAS unsigned* lw = (volatile LAS unsigned*)(lds + K2_FLAG);
        {
        const char* kg2 = ws + WS_K + (size_t)head * 65536 + (wid & 3) * 2048;
        const char* vg2 = ws + WS_V + (size_t)head * 65536 + (wid & 3) * 2048;
        const unsigned l2 = (unsigned)(uintptr_t)lds + (wid & 3) * 2048;
#pragma unroll
        for (int c = 3; c < 8; ++c) {
            const int cs = ((c + c0) & 7) * 8192;
            glds16(kg2 + cs, lane * 16, l2 + c * 8192); glds16(kg2 + cs + 1024, lane * 16, l2 + c * 8192 + 1024);
            glds16(vg2 + cs, lane * 16, l2 + 65536 + c * 8192); glds16(vg2 + cs + 1024, lane * 16, l2 + 65536 + c * 8192 + 1024);
        }
    }
        WAIT_V(0);
        if (lane == 0) __hip_atomic_fetch_add((LAS unsigned*)(lw + 10), 2u, __ATOMIC_RELAXED, __HIP_MEMORY_SCOPE_WORKGROUP);
#define M_LOAD(D, sb) asm volatile("global_load_dwordx4 %0, %1, %2" : "=v"(D) : "v"(vo), "s"(sb) : "memory")
#define M_LOAD_SC1(D, sb) asm volatile("global_load_dwordx4 %0, %1, %2 sc1" : "=v"(D) : "v"(vo), "s"(sb) : "memory")
#define M_GAP() asm volatile("s_nop 7\n\ts_nop 3")
#define M_MFMA0(C, A, B) asm volatile("v_mfma_f32_32x32x16_bf16 %0, %1, %2, 0" : "=&v"(C) : "v"(A), "v"(B))
        const char* wf = ws + WS_W2 + ((size_t)(nb * 2 + ni) * NKS_F + kh * 43) * 1024;
        const char* af = ws + WS_ACT + ((size_t)(tb * 2) * NKS_F + kh * 43) * 1024;
        const int k2o = (nb * 5 + (tb & 3) * 11) % 43;
        bf16x8 fw[K2_PF], fa0[K2_PF], fa1[K2_PF];
        {
            const int wv = nb * 4 + (wid & 3), j = wv & 1, slot = wv >> 1;
            const int ft0 = slot < 11 ? 3 * slot : 33 + 2 * (slot - 11);
            const int ft2 = slot < 11 ? ft0 + 2 : ft0 + 1;
            const char* xb = ws + WS_XF + (size_t)(tb * 2 + j) * (NKS_D * 1024);
            const char* wb0 = ws + WS_W1 + (size_t)ft0 * (NKS_D * 1024);
            const char* wb2 = ws + WS_W1 + (size_t)ft2 * (NKS_D * 1024);
            const int k1o = ((tb & 3) * 2 + j) * 4;
            bf16x8 fx[6], f0[6], f1[6], f2[6];
            int kq1 = k1o;
#define P1_ISSUE(i) do { const int kk_ = kq1 * 1024; kq1 = (kq1 + 1) & 31; \
                M_LOAD(fx[i], xb + kk_); M_LOAD(f0[i], wb0 + kk_); M_LOAD(f1[i], wb0 + NKS_D * 1024 + kk_); M_LOAD(f2[i], wb2 + kk_); } while (0)
#define P1_STEP(i, n) do { asm volatile("s_waitcnt vmcnt(" #n ")" : "+v"(fx[i]), "+v"(f0[i]), "+v"(f1[i]), "+v"(f2[i]) :: "memory"); \
                A_MFMA_ACC(c[0], f0[i], fx[i]); M_GAP(); A_MFMA_ACC(c[1], f1[i], fx[i]); M_GAP(); A_MFMA_ACC(c[2], f2[i], fx[i]); } while (0)
            P1_ISSUE(0); P1_ISSUE(1); P1_ISSUE(2); P1_ISSUE(3); P1_ISSUE(4); P1_ISSUE(5);
            f32x16 c[3];
#pragma unroll
            for (int t = 0; t < 3; ++t) {
                const unsigned* bs = (const unsigned*)(b1 + 32 * (t < 2 ? ft0 + t : ft2));
#pragma unroll
                for (int e = 0; e < 8; ++e) {
                    const int d0 = (e & 1) + 4 * (e >> 1);
                    const unsigned bw = h ? bs[d0 + 2] : bs[d0];
                    c[t][2 * e] = __uint_as_float(bw << 16); c[t][2 * e + 1] = __uint_as_float(bw & 0xffff0000u);
                }
            }
            asm volatile("s_nop 1" : "+v"(c[0]), "+v"(c[1]), "+v"(c[2]));
            for (int rd = 0; rd < 4; ++rd) {
                P1_STEP(0, 20); P1_ISSUE(0); P1_STEP(1, 20); P1_ISSUE(1); P1_STEP(2, 20); P1_ISSUE(2);
                P1_STEP(3, 20); P1_ISSUE(3); P1_STEP(4, 20); P1_ISSUE(4); P1_STEP(5, 20); P1_ISSUE(5);
            }
            P1_STEP(0, 20); P1_ISSUE(0); P1_STEP(1, 20); P1_ISSUE(1);
            P1_STEP(2, 20); P1_STEP(3, 16); P1_STEP(4, 12); P1_STEP(5, 8); P1_STEP(0, 4); P1_STEP(1, 0);
#undef P1_ISSUE
#undef P1_STEP
            {
                int kk = k2o;
#pragma unroll
                for (int k = 0; k < K2_PF; ++k) { M_LOAD(fw[k], wf + kk * 1024); kk = kk == 42 ? 0 : kk + 1; }
            }
            u32x4 gx0, gx1, gg;
            M_LOAD(gx0, x + (size_t)(tb * 64 + wv * 2) * D_); M_LOAD(gx1, x + (size_t)(tb * 64 + wv * 2 + 1) * D_); M_LOAD(gg, gw);
            asm volatile("s_nop 7\n\ts_nop 7\n\ts_nop 3" : "+v"(c[0]), "+v"(c[1]), "+v"(c[2]));
#pragma unroll
            for (int t = 0; t < 3; ++t) {
                const int ft = t < 2 ? ft0 + t : ft2;
                f32x16 a = c[t];
#pragma unroll
                for (int e = 0; e < 16; ++e) a[e] = mish_f(a[e]);
                const char* dst = ws + WS_ACT + ((size_t)(tb * 2 + j) * NKS_F + 2 * ft) * 1024;
                const u32x4 p0 = pack8(a, 0), p1 = pack8(a, 1);
                asm volatile("global_store_dwordx4 %0, %1, %2 sc1\n\ts_nop 1" :: "v"(vo), "v"(p0), "s"(dst) : "memory");
                asm volatile("global_store_dwordx4 %0, %1, %2 offset:1024 sc1\n\ts_nop 1" :: "v"(vo), "v"(p1), "s"(dst) : "memory");
            }
            {
                asm volatile("s_waitcnt vmcnt(6)" : "+v"(gx0), "+v"(gx1), "+v"(gg) :: "memory");
                float s0_ = 0.f, s1_ = 0.f;
#pragma unroll
                for (int e = 0; e < 4; ++e) {
                    const float gl = __uint_as_float(gg[e] << 16), gh = __uint_as_float(gg[e] & 0xffff0000u);
                    s0_ += __uint_as_float(gx0[e] << 16) * gl + __uint_as_float(gx0[e] & 0xffff0000u) * gh;
                    s1_ += __uint_as_float(gx1[e] << 16) * gl + __uint_as_float(gx1[e] & 0xffff0000u) * gh;
                }
                float sv = h ? s1_ : s0_;
                sv += __shfl_xor(h ? s0_ : s1_, 32);
#pragma unroll
                for (int o = 16; o > 0; o >>= 1) sv += __shfl_xor(sv, o);
                if (r == 0) __hip_atomic_store((GAS float*)(ws + WS_G) + tb * 64 + wv * 2 + h, 1.f / (1.f + __expf(-sv)), __ATOMIC_RELAXED, __HIP_MEMORY_SCOPE_AGENT);
            }
            asm volatile("s_waitcnt vmcnt(0)" : "+v"(fw[0]), "+v"(fw[1]), "+v"(fw[2]), "+v"(fw[3]), "+v"(fw[4]), "+v"(fw[5]), "+v"(fw[6]), "+v"(fw[7]) :: "memory");
            unsigned old = 0;
            if (lane == 0) old = __hip_atomic_fetch_add((LAS unsigned*)(lw + 4), 1u, __ATOMIC_RELAXED, __HIP_MEMORY_SCOPE_WORKGROUP);
            const bool last = __builtin_amdgcn_readfirstlane(old) == 3u;
            GAS unsigned* cnt = (GAS unsigned*)(ws + WS_CNT) + tb * 64;
            if (last && lane == 0) __hip_atomic_fetch_add(cnt, 1u, __ATOMIC_RELAXED, __HIP_MEMORY_SCOPE_AGENT);
            if (last) {
                for (unsigned spins = 0; spins < (1u << 22); ++spins) {
                    if ((unsigned)__builtin_amdgcn_readfirstlane(__hip_atomic_load(cnt, __ATOMIC_RELAXED, __HIP_MEMORY_SCOPE_AGENT)) >= 8u) break;
                    __builtin_amdgcn_s_sleep(4);
                }
                asm volatile("s_waitcnt vmcnt(0)" ::: "memory");
                if (lane == 0) lw[5] = 1u;
            } else {
                while (lw[5] == 0u) __builtin_amdgcn_s_sleep(2);
            }
            asm volatile("s_waitcnt vmcnt(0) lgkmcnt(0)" ::: "memory");
        }
        const int w4 = wid & 3;
        bool staged = false;
#define F_STAGE() do { const char* wsrc = (const char*)(wo + (size_t)(128 * w4) * D_ + nb * 64); \
            const unsigned wvo = ((lane >> 2) * D_ + (lane & 3) * 8) * 2; \
            const unsigned wdst = (unsigned)(uintptr_t)lds + w4 * 8192; \
            _Pragma("unroll") for (int k = 0; k < 8; ++k) { \
                glds16(wsrc + k * (16 * D_ * 2), wvo, wdst + k * 1024); \
                glds16(wsrc + k * (16 * D_ * 2) + 64, wvo, wdst + 65536 + k * 1024); } \
            staged = true; } while (0)
        f32x16 c0, c1;
        int kq2 = k2o;
#pragma unroll
        for (int k = 0; k < K2_PF; ++k) { M_LOAD_SC1(fa0[k], af + kq2 * 1024); M_LOAD_SC1(fa1[k], af + (NKS_F + kq2) * 1024); kq2 = kq2 == 42 ? 0 : kq2 + 1; }
#define P2_ISSUE(i) do { const int kk_ = kq2 * 1024; kq2 = kq2 == 42 ? 0 : kq2 + 1; \
            M_LOAD(fw[i], wf + kk_); M_LOAD_SC1(fa0[i], af + kk_); M_LOAD_SC1(fa1[i], af + NKS_F * 1024 + kk_); } while (0)
#define P2_WAIT(i, n) asm volatile("s_waitcnt vmcnt(" #n ")" : "+v"(fw[i]), "+v"(fa0[i]), "+v"(fa1[i]) :: "memory")
#define P2_STEP(i, n) do { P2_WAIT(i, n); A_MFMA_ACC(c0, fw[i], fa0[i]); M_GAP(); A_MFMA_ACC(c1, fw[i], fa1[i]); } while (0)
        P2_WAIT(0, 14); M_MFMA0(c0, fw[0], fa0[0]); M_MFMA0(c1, fw[0], fa1[0]); P2_ISSUE(0);
        P2_STEP(1, 15); P2_ISSUE(1); P2_STEP(2, 16); P2_ISSUE(2); P2_STEP(3, 17); P2_ISSUE(3);
        P2_STEP(4, 18); P2_ISSUE(4); P2_STEP(5, 19); P2_ISSUE(5); P2_STEP(6, 20); P2_ISSUE(6); P2_STEP(7, 21); P2_ISSUE(7);
        for (int rd = 0; rd < 3; ++rd) {
            if (!staged && (unsigned)__builtin_amdgcn_readfirstlane(lw[11]) >= 8u) F_STAGE();
            P2_STEP(0, 21); P2_ISSUE(0); P2_STEP(1, 21); P2_ISSUE(1); P2_STEP(2, 21); P2_ISSUE(2); P2_STEP(3, 21); P2_ISSUE(3);
            P2_STEP(4, 21); P2_ISSUE(4); P2_STEP(5, 21); P2_ISSUE(5); P2_STEP(6, 21); P2_ISSUE(6); P2_STEP(7, 21); P2_ISSUE(7);
        }
        P2_STEP(0, 21); P2_ISSUE(0); P2_STEP(1, 21); P2_ISSUE(1); P2_STEP(2, 21); P2_ISSUE(2);
        P2_STEP(3, 21); P2_STEP(4, 18); P2_STEP(5, 15); P2_STEP(6, 12); P2_STEP(7, 9); P2_STEP(0, 6); P2_STEP(1, 3); P2_STEP(2, 0);
#undef P2_ISSUE
#undef P2_WAIT
#undef P2_STEP
        asm volatile("s_nop 7\n\ts_nop 7\n\ts_nop 3" : "+v"(c0), "+v"(c1));
        LAS f32x4* red = (LAS f32x4*)(lds + K2_RED + ni * 8192);
        if (kh == 1) {
#pragma unroll
            for (int qd = 0; qd < 4; ++qd) {
                red[qd * 64 + lane] = (f32x4){c0[4 * qd], c0[4 * qd + 1], c0[4 * qd + 2], c0[4 * qd + 3]};
                red[(4 + qd) * 64 + lane] = (f32x4){c1[4 * qd], c1[4 * qd + 1], c1[4 * qd + 2], c1[4 * qd + 3]};
            }
            asm volatile("s_waitcnt lgkmcnt(0)" ::: "memory");
            if (lane == 0) lw[ni] = 1u;
        } else {
            while (lw[ni] == 0u) __builtin_amdgcn_s_sleep(2);
            asm volatile("" ::: "memory");
#pragma unroll
            for (int qd = 0; qd < 4; ++qd) {
                red[qd * 64 + lane] += (f32x4){c0[4 * qd], c0[4 * qd + 1], c0[4 * qd + 2], c0[4 * qd + 3]};
                red[(4 + qd) * 64 + lane] += (f32x4){c1[4 * qd], c1[4 * qd + 1], c1[4 * qd + 2], c1[4 * qd + 3]};
            }
            asm volatile("s_waitcnt lgkmcnt(0)" ::: "memory");
            if (lane == 0) __hip_atomic_fetch_add((LAS unsigned*)(lw + 2), 1u, __ATOMIC_RELAXED, __HIP_MEMORY_SCOPE_WORKGROUP);
        }
        if (!staged) {
            while (lw[11] < 8u) __builtin_amdgcn_s_sleep(4);
            asm volatile("" ::: "memory");
            F_STAGE();
        }
#undef F_STAGE
        float g0, g1;
        asm volatile("global_load_dword %0, %2, %3 sc1\n\tglobal_load_dword %1, %2, %3 offset:128 sc1"
                     : "=&v"(g0), "=&v"(g1) : "v"(r * 4), "s"(ws + WS_G + tb * 256) : "memory");
        {
            GAS unsigned* oc = (GAS unsigned*)(ws + WS_CNT) + (32 + (tb >> 2) * 4 + w4) * 64;
            for (unsigned spins = 0; spins < (1u << 22); ++spins) {
                if ((unsigned)__builtin_amdgcn_readfirstlane(__hip_atomic_load(oc, __ATOMIC_RELAXED, __HIP_MEMORY_SCOPE_AGENT)) >= 8u) break;
                __builtin_amdgcn_s_sleep(2);
            }
        }
        asm volatile("s_waitcnt vmcnt(0) lgkmcnt(0)" : "+v"(g0), "+v"(g1) :: "memory");
        bf16x8 fo0[8], fo1[8];
        {
            const char* ob = ws + WS_O + ((size_t)(tb * 2) * NKS_D + w4 * 8) * 1024;
#pragma unroll
            for (int k = 0; k < 8; ++k) { M_LOAD_SC1(fo0[k], ob + k * 1024); M_LOAD_SC1(fo1[k], ob + (NKS_D + k) * 1024); }
        }
        f32x16 fa[4];
        {
            const LAS char* rb = lds + w4 * 8192 + (4 * h + ((lane & 15) >> 2)) * 64 + ((lane >> 4) & 1) * 32 + (lane & 3) * 8;
#define F_STEP(k, n) do { \
                v4i16_t l0 = __builtin_amdgcn_ds_read_tr16_b64_v4i16((LAS v4i16_t*)(rb + (k) * 1024)); \
                v4i16_t h0 = __builtin_amdgcn_ds_read_tr16_b64_v4i16((LAS v4i16_t*)(rb + (k) * 1024 + 512)); \
                v4i16_t l1 = __builtin_amdgcn_ds_read_tr16_b64_v4i16((LAS v4i16_t*)(rb + 65536 + (k) * 1024)); \
                v4i16_t h1 = __builtin_amdgcn_ds_read_tr16_b64_v4i16((LAS v4i16_t*)(rb + 65536 + (k) * 1024 + 512)); \
                const bf16x8 w0_ = __builtin_bit_cast(bf16x8, __builtin_shufflevector(l0, h0, 0, 1, 2, 3, 4, 5, 6, 7)); \
                const bf16x8 w1_ = __builtin_bit_cast(bf16x8, __builtin_shufflevector(l1, h1, 0, 1, 2, 3, 4, 5, 6, 7)); \
                asm volatile("s_waitcnt vmcnt(" #n ")" : "+v"(fo0[k]), "+v"(fo1[k]) :: "memory"); \
                if ((k) == 0) { M_MFMA0(fa[0], w0_, fo0[k]); M_MFMA0(fa[1], w0_, fo1[k]); M_MFMA0(fa[2], w1_, fo0[k]); M_MFMA0(fa[3], w1_, fo1[k]); } \
                else { A_MFMA_ACC(fa[0], w0_, fo0[k]); A_MFMA_ACC(fa[1], w0_, fo1[k]); A_MFMA_ACC(fa[2], w1_, fo0[k]); A_MFMA_ACC(fa[3], w1_, fo1[k]); } \
            } while (0)
            F_STEP(0, 14); F_STEP(1, 12); F_STEP(2, 10); F_STEP(3, 8); F_STEP(4, 6); F_STEP(5, 4); F_STEP(6, 2); F_STEP(7, 0);
#undef F_STEP
        }
        asm volatile("s_nop 7\n\ts_nop 7\n\ts_nop 3" : "+v"(fa[0]), "+v"(fa[1]), "+v"(fa[2]), "+v"(fa[3]));
        while (lw[7] < 8u) __builtin_amdgcn_s_sleep(2);
        asm volatile("" ::: "memory");
        LAS f32x4* fr = (LAS f32x4*)(lds + 32768);
#define F_SLOT(w, t, qd) ((((w) & 1) * 16 + (t) * 4 + (qd)) * 64 + ((w) >> 1) * 4096 + lane)
#pragma unroll
        for (int t = 0; t < 4; ++t)
#pragma unroll
            for (int qd = 0; qd < 4; ++qd)
                fr[F_SLOT(w4, t, qd)] = (f32x4){fa[t][4 * qd], fa[t][4 * qd + 1], fa[t][4 * qd + 2], fa[t][4 * qd + 3]};
        asm volatile("s_waitcnt lgkmcnt(0)" ::: "memory");
        if (lane == 0) __hip_atomic_fetch_add((LAS unsigned*)(lw + 3), 1u, __ATOMIC_RELAXED, __HIP_MEMORY_SCOPE_WORKGROUP);
        while (lw[3] < 4u || lw[2] < 2u) __builtin_amdgcn_s_sleep(1);
        asm volatile("" ::: "memory");
        {
            const float g = (w4 & 1) ? g1 : g0;
            const LAS f32x4* bk = (const LAS f32x4*)(lds + K2_RED) + (size_t)w4 * 256 + lane;
            u16* op = out + (size_t)(tb * 64 + (w4 & 1) * 32 + r) * D_ + nb * 64 + (w4 >> 1) * 32 + 4 * h;
#pragma unroll
            for (int qd = 0; qd < 4; ++qd) {
                f32x4 sa = fr[F_SLOT(0, w4, qd)] + fr[F_SLOT(1, w4, qd)] + fr[F_SLOT(2, w4, qd)] + fr[F_SLOT(3, w4, qd)];
                sa = sa * g + bk[qd * 64] * (1.f - g);
                *(u32x2*)(op + 8 * qd) = (u32x2){pk2(sa[0], sa[1]), pk2(sa[2], sa[3])};
            }
        }
#undef F_SLOT
        return;
    }

    if (tid < 40) {
        const unsigned c = (tid & 1) ? 0u : 0x3f803f80u;
        *(LAS u32x4*)(lds + K2_CONST + (tid >> 1) * 512 + (tid & 1) * 16) = (u32x4){c, c, c, c};
    }
    const bool vact = r < 16;
    const LAS char* kp = lds + lane * 16;
    const LAS char* vp = vact ? lds + 65536 + h * 256 + r * 16 : lds + K2_CONST + (r == 16 ? 0 : 16);
    const int vinc = vact ? 8192 : 0;

    f32x16 ot = (f32x16){};
    f32x16 negm, sa, sb;
    bf16x8 ka, kb, va0, va1, vb0, vb1;
    float m;
    volatile LAS unsigned* cw = (volatile LAS unsigned*)(lds + K2_FLAG) + 8;
#define K2_SIGNAL(c) do { if (lane == 0) __hip_atomic_fetch_add((LAS unsigned*)(cw + (c)), 1u, __ATOMIC_RELAXED, __HIP_MEMORY_SCOPE_WORKGROUP); } while (0)
#define K2_NEED(c) do { while (cw[c] < 8u) __builtin_amdgcn_s_sleep(1); asm volatile("" ::: "memory"); } while (0)
    asm volatile("s_waitcnt vmcnt(0) lgkmcnt(0)" : "+v"(qf) :: "memory");
    K2_SIGNAL(1);
    K2_NEED(1);
    {
        ka = *(const LAS bf16x8*)kp;
        kb = *(const LAS bf16x8*)(kp + 1024);
        va0 = *(const LAS bf16x8*)(vp); va1 = *(const LAS bf16x8*)(vp + 512);
        f32x16 s = MFMA(ka, qf, (f32x16){});
        float mx = fmaxf(s[0], s[1]);
#pragma unroll
        for (int e = 2; e < 16; ++e) mx = fmaxf(mx, s[e]);
        mx = fmaxf(mx, __shfl_xor(mx, 32));
        m = mx;
#pragma unroll
        for (int e = 0; e < 16; ++e) negm[e] = -m;
        asm volatile("s_nop 3" : "+v"(negm));
        A_MFMA_S(sa, ka);
        asm volatile("s_nop 7\n\ts_nop 7" : "+v"(sa));
    }
    const LAS char* kq = kp;
    const LAS char* vq = vp;
    int g = 0;
    for (; g < 8; ++g) {
        if (g == 2) K2_NEED(2);
        else if (g == 4) K2_SIGNAL(3);
        if (g == 0) { if (wid >= 4) __builtin_amdgcn_s_setprio(2); else __builtin_amdgcn_s_setprio(1); }
        else if (g == 5 && wid >= 4) __builtin_amdgcn_s_setprio(0);
        const LAS char* kq2 = (g == 7) ? kp - 8192 : kq;
        const LAS char* vq2 = (g == 7) ? vp - vinc : vq;
        asm volatile("" : "+v"(kq), "+v"(vq), "+v"(kq2), "+v"(vq2));
        f32x16 og;
        float e[16];
        u32x4 p0, p1;
#define K2_TILE(SC, SN, KC, KN, VC0, VP1, VN0, VN1, t) do { \
            if ((t) > 0) A_MFMA_PV(VP1, p1); \
            KN = ((t) + 2 < 8) ? *(const LAS bf16x8*)(kq + ((t) + 2) * 1024) : *(const LAS bf16x8*)(kq2 + ((t) + 2) * 1024); \
            if ((t) + 1 < 8) { VN0 = *(const LAS bf16x8*)(vq + ((t) + 1) * 1024); VN1 = *(const LAS bf16x8*)(vq + ((t) + 1) * 1024 + 512); } \
            else { VN0 = *(const LAS bf16x8*)(vq2 + ((t) + 1) * 1024); VN1 = *(const LAS bf16x8*)(vq2 + ((t) + 1) * 1024 + 512); } \
            A_EXP4(e, SC, 0); A_EXP4(e, SC, 4); \
            A_MFMA_S(SN, KC); \
            A_CVT4(p0, e, 0); \
            A_EXP4(e, SC, 8); \
            if ((t) == 0) A_MFMA_PV0(VC0, p0); else A_MFMA_PV(VC0, p0); \
            A_EXP4(e, SC, 12); \
            A_CVT4(p1, e, 8); \
            asm volatile("s_nop 1"); \
        } while (0)
#pragma unroll
        for (int t = 0; t < 8; t += 2) {
            K2_TILE(sa, sb, kb, ka, va0, vb1, vb0, vb1, t);
            K2_TILE(sb, sa, ka, kb, vb0, va1, va0, va1, t + 1);
        }
        A_MFMA_PV(vb1, p1);
        asm volatile("s_nop 7\n\ts_nop 7\n\ts_nop 3" : "+v"(og));
#undef K2_TILE
        if (__builtin_expect(__any(!(og[8] <= K2_LIM)), 0)) break;
#pragma unroll
        for (int i = 0; i < 9; ++i) ot[i] += og[i];
        kq += 8192; vq += vinc;
    }
    if (__builtin_expect(g < 8, 0)) {
        K2_NEED(2);
        for (int t = g * 8; t < 64; ++t) {
            bf16x8 kf = *(const LAS bf16x8*)(kp + t * 1024);
            const LAS char* vt = vp + (vact ? t * 1024 : 0);
            vb0 = *(const LAS bf16x8*)(vt); vb1 = *(const LAS bf16x8*)(vt + 512);
            f32x16 s = MFMA(kf, qf, (f32x16){});
            float mx = fmaxf(s[0], s[1]);
#pragma unroll
            for (int i = 2; i < 16; ++i) mx = fmaxf(mx, s[i]);
            mx = fmaxf(mx, __shfl_xor(mx, 32));
            float mn = fmaxf(m, mx);
            float al = __builtin_amdgcn_exp2f(m - mn);
            m = mn;
            ot = ot * al;
#pragma unroll
            for (int i = 0; i < 16; ++i) s[i] = __builtin_amdgcn_exp2f(s[i] - m);
            ot = MFMA(vb0, __builtin_bit_cast(bf16x8, pack8(s, 0)), ot);
            ot = MFMA(vb1, __builtin_bit_cast(bf16x8, pack8(s, 1)), ot);
        }
        asm volatile("s_waitcnt lgkmcnt(0)" ::: "memory");
        if (g < 4) K2_SIGNAL(3);
    }
    float l = __shfl(ot[8], r);
    float rl = 1.f / l;
    f32x16 on = ot * rl;
    {
        const u32x4 po = pack8(on, 0);
        asm volatile("global_store_dwordx4 %0, %1, %2 sc1\n\ts_nop 1\n\ts_waitcnt vmcnt(0)"
                     :: "v"(lane * 16), "v"(po), "s"(ws + WS_O + ((size_t)qt * NKS_D + head) * 1024) : "memory");
        unsigned old = 0;
        if (lane == 0) old = __hip_atomic_fetch_add((LAS unsigned*)(cw - 1), 1u, __ATOMIC_RELAXED, __HIP_MEMORY_SCOPE_WORKGROUP);
        if (__builtin_amdgcn_readfirstlane(old) == 7u && lane == 0)
            __hip_atomic_fetch_add((GAS unsigned*)(ws + WS_CNT) + (32 + qb * 4 + (head >> 3)) * 64, 1u, __ATOMIC_RELAXED, __HIP_MEMORY_SCOPE_AGENT);
    }
}

extern "C" void kernel_launch(void* const* d_in, const int* in_sizes, int n_in,
                              void* d_out, int out_size, void* d_ws, size_t ws_size,
                              hipStream_t stream) {
    const u16* x = (const u16*)d_in[0];
    const u16* wqkv = (const u16*)d_in[1];
    const u16* wo = (const u16*)d_in[2];
    const u16* w1 = (const u16*)d_in[4];
    const u16* b1 = (const u16*)d_in[5];
    const u16* w2 = (const u16*)d_in[6];
    const u16* gw = (const u16*)d_in[7];
    char* ws = (char*)d_ws;
    (void)hipFuncSetAttribute((const void*)k1_kernel, hipFuncAttributeMaxDynamicSharedMemorySize, K1_LDS);
    (void)hipFuncSetAttribute((const void*)k2_kernel, hipFuncAttributeMaxDynamicSharedMemorySize, K2_LDS);
    k1_kernel<<<256, 1024, K1_LDS, stream>>>(x, wqkv, w1, w2, ws);
    k2_kernel<<<256, 768, K2_LDS, stream>>>(ws, b1, wo, x, gw, (u16*)d_out);
}
```
